```python
import torch

def _softmax(x, axis=-1):
    return torch.softmax(x, dim=axis)
def _gelu(x):
    return torch.nn.functional.gelu(x, approximate="tanh")
def _silu(x):
    return torch.nn.functional.silu(x)
def _sigmoid(x):
    return torch.sigmoid(x)
def _layernorm(x, axis=-1, eps=1e-5):
    m = x.mean(dim=axis, keepdim=True)
    v = x.var(dim=axis, keepdim=True, unbiased=False)
    return (x - m) / torch.sqrt(v + eps)
def _scatter_add(tgt, idx, src, axis=0):
    return tgt.clone().index_add_(axis, idx, src)
def _segment_sum(src, seg, n):
    out = torch.zeros((n,) + src.shape[1:], dtype=src.dtype)
    return out.index_add_(0, seg, src)
def _pad(x, pw):
    flat = tuple(v for pair in reversed(pw) for v in pair)
    return torch.nn.functional.pad(x, flat)

kernel_name = 'sequential_preLN__softmax_full_mha__glu_gated_reglu__prompt'

def setup_inputs():

    _w = (lambda a: (lambda t: t.to(torch.bfloat16) if t.is_floating_point() else t)(torch.as_tensor(a)))
    def _A_setup():
        B, S, D = 1, 2048, 512
        return {
            "x": _w((lambda *s: torch.randn(*s, dtype=torch.bfloat16) * 0.05)(B, S, D)),
            "wqkv": _w((lambda *s: torch.randn(*s, dtype=torch.bfloat16) * 0.05)(D, 3 * D)),
            "wo": _w((lambda *s: torch.randn(*s, dtype=torch.bfloat16) * 0.05)(D, D)),
        }
    _A_in = _A_setup()

    def _B_setup():
        B, S, D, F = 1, 2048, 512, 1792
        return {
            "x": _w((lambda *s: torch.randn(*s, dtype=torch.bfloat16) * 0.05)(B, S, D)),
            "W_gate": _w((lambda *s: torch.randn(*s, dtype=torch.bfloat16) * 0.4)(D, F) * (2.0 / D) ** 0.5),
            "W_up": _w((lambda *s: torch.randn(*s, dtype=torch.bfloat16) * 0.4)(D, F) * (2.0 / D) ** 0.5),
            "W_down": _w((lambda *s: torch.randn(*s, dtype=torch.bfloat16) * 0.4)(F, D) * (1.0 / F) ** 0.5),
        }
    _B_in = _B_setup()

    return {
        "A_x": _A_in["x"],
        "A_wqkv": _A_in["wqkv"],
        "A_wo": _A_in["wo"],
        "B_x": _B_in["x"],
        "B_W_gate": _B_in["W_gate"],
        "B_W_up": _B_in["W_up"],
        "B_W_down": _B_in["W_down"],
    }

def reference(A_x, A_wqkv, A_wo, B_x, B_W_gate, B_W_up, B_W_down):
    def _A_ref(x, wqkv, wo):
        B, S, D = x.shape
        H = 32; Dh = D // H
        qkv = (lambda x, *d: x.permute(*d))((x @ wqkv).reshape(B, S, 3, H, Dh), 2, 0, 3, 1, 4)
        q, k, v = qkv[0], qkv[1], qkv[2]
        scale = Dh ** -0.5
        scores = torch.einsum("bhqd,bhkd->bhqk", q, k) * scale
        p = _softmax(scores, axis=-1)
        o = torch.einsum("bhqk,bhkd->bhqd", p, v)
        return (lambda x, *d: x.permute(*d))(o, 0, 2, 1, 3).reshape(B, S, D) @ wo

    def _B_ref(x, W_gate, W_up, W_down):
        gate = torch.einsum("bsd,df->bsf", x, W_gate)
        up = torch.einsum("bsd,df->bsf", x, W_up)
        gate = gate * (lambda x, dt: x.to(dt))(gate > 0, gate.dtype)
        h = gate * up
        return torch.einsum("bsf,fd->bsd", h, W_down)

    x = A_x
    h = x + _A_ref(_layernorm(x, axis=-1), A_wqkv, A_wo)
    h2 = h + _B_ref(_layernorm(h, axis=-1), B_W_gate, B_W_up, B_W_down)
    return h2
```

```cpp
#include <hip/hip_runtime.h>
#include <hip/hip_bf16.h>
#include <stdint.h>

typedef short bf16x8 __attribute__((ext_vector_type(8)));
typedef short s16x4 __attribute__((ext_vector_type(4)));
typedef float f32x2 __attribute__((ext_vector_type(2)));
typedef float f32x4 __attribute__((ext_vector_type(4)));
typedef float f32x16 __attribute__((ext_vector_type(16)));
typedef unsigned u32x4 __attribute__((ext_vector_type(4)));
typedef unsigned u32x2 __attribute__((ext_vector_type(2)));
typedef __bf16 bf16x2n __attribute__((ext_vector_type(2)));
typedef unsigned short u16;

#define LDS_AS __attribute__((address_space(3)))
#define DEVI __device__ __forceinline__

constexpr int SEQ = 2048, DM = 512, NH = 32, DH = 16, FF = 1792;
constexpr float QSCALE = 0.25f * 1.4426950408889634f;

DEVI unsigned pk2(float lo, float hi) {
  f32x2 v = {lo, hi};
  bf16x2n r = __builtin_convertvector(v, bf16x2n);
  return __builtin_bit_cast(unsigned, r);
}
DEVI float bflo(unsigned u) { return __uint_as_float(u << 16); }
DEVI float bfhi(unsigned u) { return __uint_as_float(u & 0xffff0000u); }
DEVI float bfr(float x) { return bflo(pk2(x, x)); }

template <int N> DEVI void wait_vm() { asm volatile("s_waitcnt vmcnt(%0)" ::"n"(N) : "memory"); }
DEVI void wait_lgkm0() { asm volatile("s_waitcnt lgkmcnt(0)" ::: "memory"); }

DEVI void glds16(const void* gsrc, LDS_AS char* ldst) {
  __builtin_amdgcn_global_load_lds((const unsigned*)gsrc, (LDS_AS unsigned*)ldst, 16, 0, 0);
}
DEVI s16x4 tr_read(LDS_AS char* p) { return __builtin_amdgcn_ds_read_tr16_b64_v4i16((LDS_AS s16x4*)p); }
DEVI bf16x8 lds_read16(LDS_AS char* p) { return *(LDS_AS bf16x8*)p; }

DEVI float wave_sum(float v) {
#pragma unroll
  for (int o = 32; o >= 1; o >>= 1) v += __shfl_xor(v, o);
  return v;
}

template <int MODE>
__global__ __launch_bounds__(256, 1) void ln_gemm_kernel(const u16* __restrict__ X, const u16* __restrict__ W0,
                                                        const u16* __restrict__ W1, u16* __restrict__ O0,
                                                        u16* __restrict__ O1, u16* __restrict__ O2) {
  constexpr int NW = (MODE == 0) ? 1536 : 1792;
  constexpr int NMAT = (MODE == 0) ? 1 : 2;
  constexpr int STG = 4096 * NMAT;
  constexpr int NST = (MODE == 0) ? 6 : 3;
  constexpr int NKS = 16;
  __shared__ __attribute__((aligned(16))) char smem[65536 + 4 * 24576];
  LDS_AS char* lds = (LDS_AS char*)smem;

  const int tid = threadIdx.x, lane = tid & 63, w = __builtin_amdgcn_readfirstlane(tid >> 6);
  const int b = blockIdx.x;
  const int rb = (b & 7) * 4 + ((b >> 3) & 3), cb = b >> 5;
  const int m0 = rb * 64;
  const int n0 = cb * 256 + w * 64;

  LDS_AS char* ring = lds + 65536 + w * 24576;
  int srcoff[4];
#pragma unroll
  for (int j = 0; j < 4; ++j) {
    const int r = 8 * j + (lane >> 3), pc = lane & 7;
    const int f = ((r >> 1) & 1) | (((r >> 3) & 1) << 1);
    const int c = (pc >> 1) ^ f;
    srcoff[j] = r * NW + n0 + c * 16 + (pc & 1) * 8;
  }
  auto stage = [&](int ks) {
    LDS_AS char* dst = ring + (ks % NST) * STG;
#pragma unroll
    for (int mtx = 0; mtx < NMAT; ++mtx) {
      const u16* Wp = (mtx == 0 ? W0 : W1) + (size_t)ks * 32 * NW;
#pragma unroll
      for (int j = 0; j < 4; ++j) glds16(Wp + srcoff[j], dst + mtx * 4096 + j * 1024 + lane * 16);
    }
  };
#pragma unroll
  for (int s = 0; s < NST; ++s) stage(s);

  {
    u32x4 xr[16];
#pragma unroll
    for (int i = 0; i < 16; ++i) xr[i] = *(const u32x4*)(X + (size_t)(m0 + w * 16 + i) * DM + lane * 8);
#pragma unroll
    for (int i = 0; i < 16; ++i) {
      float v[8];
#pragma unroll
      for (int e = 0; e < 4; ++e) { v[2 * e] = bflo(xr[i][e]); v[2 * e + 1] = bfhi(xr[i][e]); }
      float s = ((v[0] + v[1]) + (v[2] + v[3])) + ((v[4] + v[5]) + (v[6] + v[7]));
      const float mean = wave_sum(s) * (1.0f / DM);
      float q = 0.f;
#pragma unroll
      for (int e = 0; e < 8; ++e) { const float d = v[e] - mean; q += d * d; }
      const float var = wave_sum(q) * (1.0f / DM);
      const float mb = bfr(mean);
      const float sd = bfr(sqrtf(bfr(bfr(var) + 1e-5f)));
      const float inv = 1.0f / sd;
      u32x4 y;
#pragma unroll
      for (int e = 0; e < 4; ++e) {
        const float a = bfr(v[2 * e] - mb) * inv, c = bfr(v[2 * e + 1] - mb) * inv;
        y[e] = pk2(a, c);
      }
      const int row = w * 16 + i;
      *(LDS_AS u32x4*)(lds + row * 1024 + ((lane ^ (row & 15)) * 16)) = y;
    }
  }
  wait_lgkm0();
  __builtin_amdgcn_s_barrier();
  asm volatile("" ::: "memory");

  const int fr = lane & 15, g = lane >> 4, q4 = (lane & 15) >> 2, p4 = lane & 3;
  f32x4 acc[NMAT][4][4];
#pragma unroll
  for (int a = 0; a < NMAT; ++a)
#pragma unroll
    for (int i = 0; i < 4; ++i)
#pragma unroll
      for (int j = 0; j < 4; ++j) acc[a][i][j] = (f32x4){0.f, 0.f, 0.f, 0.f};
  const int rB = 8 * g + q4;
  const int fB = ((rB >> 1) & 1) | (((rB >> 3) & 1) << 1);
  int boff[4];
#pragma unroll
  for (int nt = 0; nt < 4; ++nt) boff[nt] = rB * 128 + ((nt ^ fB) * 32) + 8 * p4;
  const int aoff = fr * 1024;

#pragma unroll
  for (int ks = 0; ks < NKS; ++ks) {
    constexpr int L = 4 * NMAT;
    if (ks + NST <= NKS) wait_vm<(NST - 1) * L>();
    else {
      if (NKS - 1 - ks == 0) wait_vm<0>();
      else if (NKS - 1 - ks == 1) wait_vm<1 * L>();
      else if (NKS - 1 - ks == 2) wait_vm<2 * L>();
      else if (NKS - 1 - ks == 3) wait_vm<3 * L>();
      else wait_vm<4 * L>();
    }
    LDS_AS char* st = ring + (ks % NST) * STG;
    bf16x8 af[4];
#pragma unroll
    for (int mt = 0; mt < 4; ++mt) af[mt] = lds_read16(lds + aoff + mt * 16384 + (((4 * ks + g) ^ fr) * 16));
    bf16x8 bfg[NMAT][4];
#pragma unroll
    for (int mtx = 0; mtx < NMAT; ++mtx)
#pragma unroll
      for (int nt = 0; nt < 4; ++nt) {
        const s16x4 lo = tr_read(st + mtx * 4096 + boff[nt]);
        const s16x4 hi = tr_read(st + mtx * 4096 + boff[nt] + 512);
        bfg[mtx][nt] = __builtin_shufflevector(lo, hi, 0, 1, 2, 3, 4, 5, 6, 7);
      }
#pragma unroll
    for (int mtx = 0; mtx < NMAT; ++mtx)
#pragma unroll
      for (int mt = 0; mt < 4; ++mt)
#pragma unroll
        for (int nt = 0; nt < 4; ++nt)
          acc[mtx][mt][nt] = __builtin_amdgcn_mfma_f32_16x16x32_bf16(bfg[mtx][nt], af[mt], acc[mtx][mt][nt], 0, 0, 0);
    if (ks + NST < NKS) {
      wait_lgkm0();
      stage(ks + NST);
    }
  }

#pragma unroll
  for (int mt = 0; mt < 4; ++mt) {
    const int tok = m0 + 16 * mt + fr;
#pragma unroll
    for (int nt = 0; nt < 4; ++nt) {
      const int col = n0 + 16 * nt + 4 * g;
      if constexpr (MODE == 0) {
        const int which = col >> 9, head = (col >> 4) & 31;
        f32x4 v = acc[0][mt][nt];
        u16* dst = (which == 0 ? O0 : (which == 1 ? O1 : O2));
        if (which == 0) v = v * QSCALE;
        u32x2 o = {pk2(v[0], v[1]), pk2(v[2], v[3])};
        *(u32x2*)(dst + ((size_t)head * SEQ + tok) * DH + 4 * g) = o;
      } else {
        const f32x4 gt = acc[0][mt][nt], up = acc[1][mt][nt];
        float r[4];
#pragma unroll
        for (int e = 0; e < 4; ++e) {
          const float gg = bfr(gt[e]), uu = bfr(up[e]);
          r[e] = gg > 0.f ? gg * uu : 0.f;
        }
        u32x2 o = {pk2(r[0], r[1]), pk2(r[2], r[3])};
        *(u32x2*)(O0 + (size_t)tok * FF + col) = o;
      }
    }
  }
}

constexpr int AT_K = 0, AT_V = 65536, AT_C = 131072;
__global__ __launch_bounds__(512, 1) void attn_kernel(const u16* __restrict__ Qh, const u16* __restrict__ Kh,
                                                      const u16* __restrict__ Vh, u16* __restrict__ AO) {
  __shared__ __attribute__((aligned(16))) char smem[131072 + 1024];
  LDS_AS char* lds = (LDS_AS char*)smem;
  const int tid = threadIdx.x, lane = tid & 63, w = __builtin_amdgcn_readfirstlane(tid >> 6);
  const int b = blockIdx.x;
  const int head = (b & 7) * 4 + (b >> 6), qb = (b >> 3) & 7;
  const u16* Kp = Kh + (size_t)head * SEQ * DH;
  const u16* Vp = Vh + (size_t)head * SEQ * DH;

  const int l31 = lane & 31, hh = lane >> 5;
  const int q0 = qb * 256 + w * 32;
  const bf16x8 qf = *(const bf16x8*)(Qh + ((size_t)head * SEQ + q0 + l31) * DH + 8 * hh);
#pragma unroll
  for (int j = 0; j < 8; ++j) {
    const int o = (w * 8 + j) * 1024 + lane * 16;
    const int key = o >> 5, piece = ((o >> 4) & 1) ^ ((key >> 3) & 1);
    glds16(Kp + key * 16 + piece * 8, lds + AT_K + o);
  }
#pragma unroll
  for (int j = 0; j < 8; ++j) {
    const int o = (w * 8 + j) * 1024 + lane * 16;
    glds16(Vp + (o >> 1), lds + AT_V + o);
  }
  if (tid < 64) {
    const unsigned one = (lane & 1) ? 0u : 0x3f80u;
    u32x4 v = {one, 0u, 0u, 0u};
    *(LDS_AS u32x4*)(lds + AT_C + lane * 16) = v;
  }
  const int koff = AT_K + l31 * 32 + ((hh ^ ((l31 >> 3) & 1)) * 16);

  wait_vm<8>();
  wait_lgkm0();
  __builtin_amdgcn_s_barrier();
  asm volatile("" ::: "memory");

  float mx = -3.0e38f;
  {
    const f32x16 zero = {};
#pragma unroll 4
    for (int t = 0; t < 64; ++t) {
      const bf16x8 kf = lds_read16(lds + koff + t * 1024);
      const f32x16 x = __builtin_amdgcn_mfma_f32_32x32x16_bf16(kf, qf, zero, 0, 0, 0);
#pragma unroll
      for (int r = 0; r < 16; r += 2) mx = fmaxf(fmaxf(mx, x[r]), x[r + 1]);
    }
    auto rr = __builtin_amdgcn_permlane32_swap(__float_as_uint(mx), __float_as_uint(mx), false, false);
    mx = fmaxf(__uint_as_float(rr[0]), __uint_as_float(rr[1]));
  }
  wait_vm<0>();
  __builtin_amdgcn_s_barrier();
  asm volatile("" ::: "memory");

  f32x16 o = {};
  f32x16 cinit;
#pragma unroll
  for (int r = 0; r < 16; ++r) cinit[r] = -mx;
  const int up = (lane >> 4) & 1, q4 = (lane & 15) >> 2, p4 = lane & 3;
  int voff = (up ? (AT_C + 128 * (1 - hh)) : (AT_V + 128 * hh)) + q4 * 32 + 8 * p4;
  const int vinc = up ? 0 : 1024;
#pragma unroll 2
  for (int t = 0; t < 64; ++t) {
    const bf16x8 kf = lds_read16(lds + koff + t * 1024);
    const f32x16 x = __builtin_amdgcn_mfma_f32_32x32x16_bf16(kf, qf, cinit, 0, 0, 0);
    float pe[16];
#pragma unroll
    for (int r = 0; r < 16; ++r) pe[r] = __builtin_amdgcn_exp2f(x[r]);
#pragma unroll
    for (int s = 0; s < 2; ++s) {
      u32x4 pk;
#pragma unroll
      for (int e = 0; e < 4; ++e) pk[e] = pk2(pe[8 * s + 2 * e], pe[8 * s + 2 * e + 1]);
      const s16x4 lo = tr_read(lds + voff + s * 512);
      const s16x4 hi = tr_read(lds + voff + s * 512 + 256);
      const bf16x8 vf = __builtin_shufflevector(lo, hi, 0, 1, 2, 3, 4, 5, 6, 7);
      o = __builtin_amdgcn_mfma_f32_32x32x16_bf16(vf, __builtin_bit_cast(bf16x8, pk), o, 0, 0, 0);
    }
    voff += vinc;
  }
  auto ls = __builtin_amdgcn_permlane32_swap(__float_as_uint(o[8]), __float_as_uint(o[8]), false, false);
  const float linv = 1.0f / __uint_as_float(ls[0]);
  u16* dst = AO + (size_t)(q0 + l31) * DM + head * DH + 4 * hh;
  u32x2 o0 = {pk2(o[0] * linv, o[1] * linv), pk2(o[2] * linv, o[3] * linv)};
  u32x2 o1 = {pk2(o[4] * linv, o[5] * linv), pk2(o[6] * linv, o[7] * linv)};
  *(u32x2*)(dst) = o0;
  *(u32x2*)(dst + 8) = o1;
}

template <int KT>
__global__ __launch_bounds__(256, 1) void gemm_res_kernel(const u16* __restrict__ A, const u16* __restrict__ W,
                                                         const u16* __restrict__ RES, u16* __restrict__ OUT) {
  constexpr int KW = KT / 4;
  constexpr int NSTG = KW / 64;
  __shared__ __attribute__((aligned(16))) char smem[4 * 32768];
  LDS_AS char* lds = (LDS_AS char*)smem;
  const int tid = threadIdx.x, lane = tid & 63, w = __builtin_amdgcn_readfirstlane(tid >> 6);
  const int b = blockIdx.x;
  const int rb = (b & 7) * 4 + ((b >> 3) & 3), cb = b >> 5;
  const int m0 = rb * 64, n0 = cb * 64;
  LDS_AS char* ring = lds + w * 32768;

  int aoffs[8], boffs[8];
#pragma unroll
  for (int j = 0; j < 8; ++j) {
    const int r = 8 * j + (lane >> 3), pc = lane & 7;
    aoffs[j] = (m0 + r) * KT + ((pc ^ ((r >> 1) & 7)) * 8);
    const int f = ((r >> 1) & 1) | (((r >> 3) & 1) << 1);
    boffs[j] = r * DM + n0 + (((pc >> 1) ^ f) * 16) + (pc & 1) * 8;
  }
  auto stage = [&](int s) {
    LDS_AS char* dst = ring + (s & 1) * 16384;
    const int k0 = w * KW + s * 64;
#pragma unroll
    for (int j = 0; j < 8; ++j) glds16(A + aoffs[j] + k0, dst + j * 1024 + lane * 16);
#pragma unroll
    for (int j = 0; j < 8; ++j) glds16(W + (size_t)k0 * DM + boffs[j], dst + 8192 + j * 1024 + lane * 16);
  };
  stage(0);
  if (NSTG > 1) stage(1);

  const int fr = lane & 15, g = lane >> 4, q4 = (lane & 15) >> 2, p4 = lane & 3;
  f32x4 acc[4][4];
#pragma unroll
  for (int i = 0; i < 4; ++i)
#pragma unroll
    for (int j = 0; j < 4; ++j) acc[i][j] = (f32x4){0.f, 0.f, 0.f, 0.f};
  const int rB = 8 * g + q4;
  const int fB = ((rB >> 1) & 1) | (((rB >> 3) & 1) << 1);

#pragma unroll
  for (int s = 0; s < NSTG; ++s) {
    if (s + 1 < NSTG) wait_vm<16>(); else wait_vm<0>();
    LDS_AS char* st = ring + (s & 1) * 16384;
#pragma unroll
    for (int k2 = 0; k2 < 2; ++k2) {
      bf16x8 af[4], bfg[4];
#pragma unroll
      for (int mt = 0; mt < 4; ++mt) {
        const int row = 16 * mt + fr;
        af[mt] = lds_read16(st + row * 128 + (((4 * k2 + g) ^ ((row >> 1) & 7)) * 16));
      }
#pragma unroll
      for (int nt = 0; nt < 4; ++nt) {
        const int o = 8192 + (32 * k2 + rB) * 128 + ((nt ^ fB) * 32) + 8 * p4;
        const s16x4 lo = tr_read(st + o);
        const s16x4 hi = tr_read(st + o + 512);
        bfg[nt] = __builtin_shufflevector(lo, hi, 0, 1, 2, 3, 4, 5, 6, 7);
      }
#pragma unroll
      for (int mt = 0; mt < 4; ++mt)
#pragma unroll
        for (int nt = 0; nt < 4; ++nt)
          acc[mt][nt] = __builtin_amdgcn_mfma_f32_16x16x32_bf16(bfg[nt], af[mt], acc[mt][nt], 0, 0, 0);
    }
    if (s + 2 < NSTG) {
      wait_lgkm0();
      stage(s + 2);
    }
  }
#pragma unroll
  for (int mt = 0; mt < 4; ++mt)
#pragma unroll
    for (int nt = 0; nt < 4; ++nt)
      *(LDS_AS f32x4*)(ring + (16 * mt + fr) * 256 + (16 * nt + 4 * g) * 4) = acc[mt][nt];
  __syncthreads();
  {
    const int row = tid >> 2, c0 = (tid & 3) * 16;
    float s[16];
#pragma unroll
    for (int e = 0; e < 16; ++e) s[e] = 0.f;
#pragma unroll
    for (int ww = 0; ww < 4; ++ww)
#pragma unroll
      for (int e4 = 0; e4 < 4; ++e4) {
        const f32x4 v = *(LDS_AS f32x4*)(lds + ww * 32768 + row * 256 + (c0 + 4 * e4) * 4);
        s[4 * e4] += v[0]; s[4 * e4 + 1] += v[1]; s[4 * e4 + 2] += v[2]; s[4 * e4 + 3] += v[3];
      }
    const size_t go = (size_t)(m0 + row) * DM + n0 + c0;
    const u32x4 r0 = *(const u32x4*)(RES + go), r1 = *(const u32x4*)(RES + go + 8);
    u32x4 o0, o1;
#pragma unroll
    for (int e = 0; e < 4; ++e) {
      o0[e] = pk2(bflo(r0[e]) + bfr(s[2 * e]), bfhi(r0[e]) + bfr(s[2 * e + 1]));
      o1[e] = pk2(bflo(r1[e]) + bfr(s[8 + 2 * e]), bfhi(r1[e]) + bfr(s[8 + 2 * e + 1]));
    }
    *(u32x4*)(OUT + go) = o0;
    *(u32x4*)(OUT + go + 8) = o1;
  }
}

extern "C" void kernel_launch(void* const* d_in, const int* in_sizes, int n_in, void* d_out, int out_size,
                              void* d_ws, size_t ws_size, hipStream_t stream) {
  const u16* x = (const u16*)d_in[0];
  const u16* wqkv = (const u16*)d_in[1];
  const u16* wo = (const u16*)d_in[2];
  const u16* wg = (const u16*)d_in[4];
  const u16* wu = (const u16*)d_in[5];
  const u16* wd = (const u16*)d_in[6];
  u16* out = (u16*)d_out;
  char* ws = (char*)d_ws;
  const size_t MB2 = 2u << 20;
  u16* Qh = (u16*)(ws);
  u16* Kh = (u16*)(ws + MB2);
  u16* Vh = (u16*)(ws + 2 * MB2);
  u16* AO = (u16*)(ws + 3 * MB2);
  u16* Hb = (u16*)(ws + 4 * MB2);
  u16* Act = (u16*)(ws + 5 * MB2);

  ln_gemm_kernel<0><<<192, 256, 0, stream>>>(x, wqkv, wqkv, Qh, Kh, Vh);
  attn_kernel<<<256, 512, 0, stream>>>(Qh, Kh, Vh, AO);
  gemm_res_kernel<512><<<256, 256, 0, stream>>>(AO, wo, x, Hb);
  ln_gemm_kernel<1><<<224, 256, 0, stream>>>(Hb, wg, wu, Act, Act, Act);
  gemm_res_kernel<1792><<<256, 256, 0, stream>>>(Act, wd, Hb, out);
}
```

```cpp
#include <hip/hip_runtime.h>
#include <hip/hip_bf16.h>
#include <stdint.h>

typedef short bf16x8 __attribute__((ext_vector_type(8)));
typedef short s16x4 __attribute__((ext_vector_type(4)));
typedef float f32x2 __attribute__((ext_vector_type(2)));
typedef float f32x4 __attribute__((ext_vector_type(4)));
typedef float f32x16 __attribute__((ext_vector_type(16)));
typedef unsigned u32x4 __attribute__((ext_vector_type(4)));
typedef unsigned u32x2 __attribute__((ext_vector_type(2)));
typedef __bf16 bf16x2n __attribute__((ext_vector_type(2)));
typedef unsigned short u16;

#define LDS_AS __attribute__((address_space(3)))
#define DEVI __device__ __forceinline__

constexpr int SEQ = 2048, DM = 512, NH = 32, DH = 16, FF = 1792;
constexpr float QSCALE = 0.25f * 1.4426950408889634f;

DEVI unsigned pk2(float lo, float hi) {
  f32x2 v = {lo, hi};
  bf16x2n r = __builtin_convertvector(v, bf16x2n);
  return __builtin_bit_cast(unsigned, r);
}
DEVI float bflo(unsigned u) { return __uint_as_float(u << 16); }
DEVI float bfhi(unsigned u) { return __uint_as_float(u & 0xffff0000u); }
DEVI float bfr(float x) { return bflo(pk2(x, x)); }

template <int N> DEVI void wait_vm() { asm volatile("s_waitcnt vmcnt(%0)" ::"n"(N) : "memory"); }
DEVI void wait_lgkm0() { asm volatile("s_waitcnt lgkmcnt(0)" ::: "memory"); }

DEVI void glds16(const void* gsrc, unsigned lds_base) {
  unsigned keep;
  asm volatile("s_mov_b32 %0, m0\n\ts_mov_b32 m0, %2\n\ts_nop 0\n\tglobal_load_lds_dwordx4 %1, off\n\ts_mov_b32 m0, %0"
               : "=&s"(keep) : "v"(gsrc), "s"(lds_base) : "memory");
}
DEVI unsigned lds_addr(LDS_AS char* p) { return (unsigned)(uintptr_t)p; }
DEVI float quad_sum(float v) {
  v += __int_as_float(__builtin_amdgcn_mov_dpp(__float_as_int(v), 0xB1, 0xF, 0xF, true));
  v += __int_as_float(__builtin_amdgcn_mov_dpp(__float_as_int(v), 0x4E, 0xF, 0xF, true));
  return v;
}
DEVI s16x4 tr_read(LDS_AS char* p) { return __builtin_amdgcn_ds_read_tr16_b64_v4i16((LDS_AS s16x4*)p); }
DEVI bf16x8 lds_read16(LDS_AS char* p) { return *(LDS_AS bf16x8*)p; }

#define TS_DECL
#define TS_MARK(i)
#define TS_END(kid)

template <int MODE>
__global__ __launch_bounds__(512, 1) void ln_gemm_kernel(const u16* __restrict__ X, const u16* __restrict__ W0,
                                                        const u16* __restrict__ W1, u16* __restrict__ O0,
                                                        u16* __restrict__ O1, u16* __restrict__ O2) {
  constexpr int NW = (MODE == 0) ? 1536 : 1792;
  constexpr int NMAT = (MODE == 0) ? 1 : 2;
  constexpr int STG = 16384 * NMAT;
  constexpr int NST = (MODE == 0) ? 6 : 3;
  constexpr int NKS = 16;
  constexpr int L = 4 * NMAT;
  __shared__ __attribute__((aligned(16))) char smem[65536 + 98304];
  LDS_AS char* lds = (LDS_AS char*)smem;

  TS_DECL
  const int tid = threadIdx.x, lane = tid & 63, wv = __builtin_amdgcn_readfirstlane(tid >> 6);
  const int w = wv & 3;
  const int b = blockIdx.x;
  const int rb = (b & 7) * 4 + ((b >> 3) & 3), cb = b >> 5;
  const int m0 = rb * 64;
  const int n0 = cb * 256 + w * 64;
  LDS_AS char* ring = lds + 65536 + w * (4096 * NMAT);

  if (wv >= 4) {
    int srcoff[4];
#pragma unroll
    for (int j = 0; j < 4; ++j) {
      const int r = 8 * j + (lane >> 3), pc = lane & 7;
      const int f = ((r >> 1) & 1) | (((r >> 3) & 1) << 1);
      const int c = (pc >> 1) ^ f;
      srcoff[j] = r * NW + n0 + c * 16 + (pc & 1) * 8;
    }
    auto stage = [&](int ks) {
      const unsigned dst = lds_addr(ring) + (ks % NST) * STG;
#pragma unroll
      for (int mtx = 0; mtx < NMAT; ++mtx) {
        const u16* Wp = (mtx == 0 ? W0 : W1) + (size_t)ks * 32 * NW;
#pragma unroll
        for (int j = 0; j < 4; ++j) glds16(Wp + srcoff[j], dst + mtx * 4096 + j * 1024);
      }
    };
    __builtin_amdgcn_s_barrier();
    asm volatile("" ::: "memory");
#pragma unroll
    for (int s = 0; s < NST - 1; ++s) stage(s);
#pragma unroll
    for (int i = 0; i < NKS; ++i) {
      if (i + NST - 2 < NKS) wait_vm<(NST - 2) * L>();
      else if (NKS - 1 - i == 0) wait_vm<0>();
      else if (NKS - 1 - i == 1) wait_vm<1 * L>();
      else if (NKS - 1 - i == 2) wait_vm<2 * L>();
      else wait_vm<3 * L>();
      __builtin_amdgcn_s_barrier();
      asm volatile("" ::: "memory");
      if (i + NST - 1 < NKS) stage(i + NST - 1);
    }
    __builtin_amdgcn_s_barrier();
    return;
  }

  {
    const int row = w * 16 + (lane >> 2), part = lane & 3;
    const u16* xp = X + (size_t)(m0 + row) * DM + part * 8;
    u32x4 xr[16];
#pragma unroll
    for (int j = 0; j < 16; ++j) xr[j] = *(const u32x4*)(xp + j * 32);
    asm volatile("" ::: "memory");
    __builtin_amdgcn_s_barrier();
    asm volatile("" ::: "memory");
    const float x0 = __int_as_float(__builtin_amdgcn_mov_dpp((int)(xr[0][0] << 16), 0x00, 0xF, 0xF, true));
    const f32x2 piv = {x0, x0};
    f32x2 v[64];
    f32x2 sa = {0.f, 0.f}, sq = {0.f, 0.f};
#pragma unroll
    for (int j = 0; j < 16; ++j)
#pragma unroll
      for (int e = 0; e < 4; ++e) {
        f32x2 d = (f32x2){bflo(xr[j][e]), bfhi(xr[j][e])} - piv;
        v[4 * j + e] = d;
        sa += d;
        sq += d * d;
      }
    const float s1 = quad_sum(sa[0] + sa[1]) * (1.0f / DM);
    const float s2 = quad_sum(sq[0] + sq[1]) * (1.0f / DM);
    const float mean = x0 + s1;
    const float var = fmaxf(s2 - s1 * s1, 0.f);
    const float mb = bfr(mean);
    const float sd = bfr(sqrtf(bfr(bfr(var) + 1e-5f)));
    const float inv = 1.0f / sd;
    const f32x2 inv2 = {inv, inv}, c2 = {(x0 - mb) * inv, (x0 - mb) * inv};
#pragma unroll
    for (int j = 0; j < 16; ++j) {
      u32x4 y;
#pragma unroll
      for (int e = 0; e < 4; ++e) {
        const f32x2 r = v[4 * j + e] * inv2 + c2;
        y[e] = pk2(r[0], r[1]);
      }
      *(LDS_AS u32x4*)(lds + row * 1024 + (((part + 4 * j) ^ (row & 15)) * 16)) = y;
    }
  }
  TS_MARK(0);

  const int fr = lane & 15, g = lane >> 4, q4 = (lane & 15) >> 2, p4 = lane & 3;
  f32x4 acc[NMAT][4][4];
#pragma unroll
  for (int a = 0; a < NMAT; ++a)
#pragma unroll
    for (int i = 0; i < 4; ++i)
#pragma unroll
      for (int j = 0; j < 4; ++j) acc[a][i][j] = (f32x4){0.f, 0.f, 0.f, 0.f};
  const int rB = 8 * g + q4;
  const int fB = ((rB >> 1) & 1) | (((rB >> 3) & 1) << 1);
  int boff[4];
#pragma unroll
  for (int nt = 0; nt < 4; ++nt) boff[nt] = rB * 128 + ((nt ^ fB) * 32) + 8 * p4;
  const int aoff = fr * 1024;
  bf16x8 af[2][4], bfg[2][NMAT][4];
  auto load_frags = [&](int ks, int buf) {
    LDS_AS char* st = ring + (ks % NST) * STG;
#pragma unroll
    for (int mt = 0; mt < 4; ++mt) af[buf][mt] = lds_read16(lds + aoff + mt * 16384 + (((4 * ks + g) ^ fr) * 16));
#pragma unroll
    for (int mtx = 0; mtx < NMAT; ++mtx)
#pragma unroll
      for (int nt = 0; nt < 4; ++nt) {
        const s16x4 lo = tr_read(st + mtx * 4096 + boff[nt]);
        const s16x4 hi = tr_read(st + mtx * 4096 + boff[nt] + 512);
        bfg[buf][mtx][nt] = __builtin_shufflevector(lo, hi, 0, 1, 2, 3, 4, 5, 6, 7);
      }
  };
#pragma unroll
  for (int i = 0; i <= NKS; ++i) {
    wait_lgkm0();
    __builtin_amdgcn_s_barrier();
    asm volatile("" ::: "memory");

    if (i < NKS) load_frags(i, i & 1);
    if (i > 0) {
#pragma unroll
      for (int mtx = 0; mtx < NMAT; ++mtx)
#pragma unroll
        for (int mt = 0; mt < 4; ++mt)
#pragma unroll
          for (int nt = 0; nt < 4; ++nt)
            acc[mtx][mt][nt] = __builtin_amdgcn_mfma_f32_16x16x32_bf16(bfg[(i - 1) & 1][mtx][nt], af[(i - 1) & 1][mt], acc[mtx][mt][nt], 0, 0, 0);
      if (i < NKS) {
#pragma unroll
        for (int q = 0; q < 4 + 8 * NMAT; ++q) {
          __builtin_amdgcn_sched_group_barrier(0x008, 1, 0);
          __builtin_amdgcn_sched_group_barrier(0x100, 1, 0);
        }
        __builtin_amdgcn_sched_group_barrier(0x008, 16 * NMAT, 0);
      }
    }
    __builtin_amdgcn_sched_barrier(0);
  }
  TS_MARK(1);

#pragma unroll
  for (int mt = 0; mt < 4; ++mt) {
    const int tok = m0 + 16 * mt + fr;
#pragma unroll
    for (int nt = 0; nt < 4; ++nt) {
      const int col = n0 + 16 * nt + 4 * g;
      if constexpr (MODE == 0) {
        const int which = col >> 9, head = (col >> 4) & 31;
        f32x4 v = acc[0][mt][nt];
        u16* dst = (which == 0 ? O0 : (which == 1 ? O1 : O2));
        if (which == 0) v = v * QSCALE;
        u32x2 o = {pk2(v[0], v[1]), pk2(v[2], v[3])};
        *(u32x2*)(dst + ((size_t)head * SEQ + tok) * DH + 4 * g) = o;
      } else {
        const f32x4 gt = acc[0][mt][nt], up = acc[1][mt][nt];
        float r[4];
#pragma unroll
        for (int e = 0; e < 4; ++e) r[e] = fmaxf(gt[e], 0.f) * up[e];
        u32x2 o = {pk2(r[0], r[1]), pk2(r[2], r[3])};
        *(u32x2*)(O0 + (size_t)tok * FF + col) = o;
      }
    }
  }
  TS_MARK(2);
  TS_END(MODE == 0 ? 0 : 3);
}

constexpr int AT_K = 0, AT_V = 65536, AT_C = 131072;
__global__ __launch_bounds__(512, 1) void attn_kernel(const u16* __restrict__ Qh, const u16* __restrict__ Kh,
                                                      const u16* __restrict__ Vh, u16* __restrict__ AO) {
  __shared__ __attribute__((aligned(16))) char smem[131072 + 1024];
  LDS_AS char* lds = (LDS_AS char*)smem;
  const int tid = threadIdx.x, lane = tid & 63, w = __builtin_amdgcn_readfirstlane(tid >> 6);
  const int b = blockIdx.x;
  TS_DECL
  const int head = (b & 7) * 4 + (b >> 6), qb = (b >> 3) & 7;
  const u16* Kp = Kh + (size_t)head * SEQ * DH;
  const u16* Vp = Vh + (size_t)head * SEQ * DH;

  const int l31 = lane & 31, hh = lane >> 5;
  const int q0 = qb * 256 + w * 32;
  const bf16x8 qf = *(const bf16x8*)(Qh + ((size_t)head * SEQ + q0 + l31) * DH + 8 * hh);
#pragma unroll
  for (int j = 0; j < 8; ++j) {
    const int o = (w * 8 + j) * 1024 + lane * 16;
    const int key = o >> 5, piece = ((o >> 4) & 1) ^ ((key >> 3) & 1);
    glds16(Kp + key * 16 + piece * 8, lds_addr(lds) + AT_K + (w * 8 + j) * 1024);
  }
#pragma unroll
  for (int j = 0; j < 8; ++j) {
    const int o = (w * 8 + j) * 1024 + lane * 16;
    glds16(Vp + (o >> 1), lds_addr(lds) + AT_V + (w * 8 + j) * 1024);
  }
  if (tid < 64) {
    const unsigned one = (lane & 1) ? 0u : 0x3f80u;
    u32x4 v = {one, 0u, 0u, 0u};
    *(LDS_AS u32x4*)(lds + AT_C + lane * 16) = v;
  }
  const int koff = AT_K + l31 * 32 + ((hh ^ ((l31 >> 3) & 1)) * 16);

  wait_vm<8>();
  wait_lgkm0();
  __builtin_amdgcn_s_barrier();
  asm volatile("" ::: "memory");

  TS_MARK(0);
  float mx = -3.0e38f;
  {
    const f32x16 zero = {};
#pragma unroll 4
    for (int t = 0; t < 64; ++t) {
      const bf16x8 kf = lds_read16(lds + koff + t * 1024);
      const f32x16 x = __builtin_amdgcn_mfma_f32_32x32x16_bf16(kf, qf, zero, 0, 0, 0);
#pragma unroll
      for (int r = 0; r < 16; r += 2) mx = fmaxf(fmaxf(mx, x[r]), x[r + 1]);
    }
    auto rr = __builtin_amdgcn_permlane32_swap(__float_as_uint(mx), __float_as_uint(mx), false, false);
    mx = fmaxf(__uint_as_float(rr[0]), __uint_as_float(rr[1]));
  }
  wait_vm<0>();
  __builtin_amdgcn_s_barrier();
  asm volatile("" ::: "memory");

  TS_MARK(1);
  f32x16 o = {};
  f32x16 cinit;
#pragma unroll
  for (int r = 0; r < 16; ++r) cinit[r] = -mx;
  const int up = (lane >> 4) & 1, q4 = (lane & 15) >> 2, p4 = lane & 3;
  int voff = (up ? (AT_C + 128 * (1 - hh)) : (AT_V + 128 * hh)) + q4 * 32 + 8 * p4;
  const int vinc = up ? 0 : 1024;
#pragma unroll 2
  for (int t = 0; t < 64; ++t) {
    const bf16x8 kf = lds_read16(lds + koff + t * 1024);
    const f32x16 x = __builtin_amdgcn_mfma_f32_32x32x16_bf16(kf, qf, cinit, 0, 0, 0);
    float pe[16];
#pragma unroll
    for (int r = 0; r < 16; ++r) pe[r] = __builtin_amdgcn_exp2f(x[r]);
#pragma unroll
    for (int s = 0; s < 2; ++s) {
      u32x4 pk;
#pragma unroll
      for (int e = 0; e < 4; ++e) pk[e] = pk2(pe[8 * s + 2 * e], pe[8 * s + 2 * e + 1]);
      const s16x4 lo = tr_read(lds + voff + s * 512);
      const s16x4 hi = tr_read(lds + voff + s * 512 + 256);
      const bf16x8 vf = __builtin_shufflevector(lo, hi, 0, 1, 2, 3, 4, 5, 6, 7);
      o = __builtin_amdgcn_mfma_f32_32x32x16_bf16(vf, __builtin_bit_cast(bf16x8, pk), o, 0, 0, 0);
    }
    voff += vinc;
  }
  TS_MARK(2);
  auto ls = __builtin_amdgcn_permlane32_swap(__float_as_uint(o[8]), __float_as_uint(o[8]), false, false);
  const float linv = 1.0f / __uint_as_float(ls[0]);
  u16* dst = AO + (size_t)(q0 + l31) * DM + head * DH + 4 * hh;
  u32x2 o0 = {pk2(o[0] * linv, o[1] * linv), pk2(o[2] * linv, o[3] * linv)};
  u32x2 o1 = {pk2(o[4] * linv, o[5] * linv), pk2(o[6] * linv, o[7] * linv)};
  *(u32x2*)(dst) = o0;
  *(u32x2*)(dst + 8) = o1;
  TS_MARK(3);
  TS_END(1);
}

template <int KT>
__global__ __launch_bounds__(512, 1) void gemm_res_kernel(const u16* __restrict__ A, const u16* __restrict__ W,
                                                         const u16* __restrict__ RES, u16* __restrict__ OUT) {
  constexpr int NSTG = KT / 128;
  constexpr int NST = 4;
  constexpr int STG = 32768;
  __shared__ __attribute__((aligned(16))) char smem[NST * STG];
  LDS_AS char* lds = (LDS_AS char*)smem;
  TS_DECL
  const int tid = threadIdx.x, lane = tid & 63, wv = __builtin_amdgcn_readfirstlane(tid >> 6);
  const int w = wv & 3;
  const int b = blockIdx.x;
  const int rb = (b & 7) * 4 + ((b >> 3) & 3), cb = b >> 5;
  const int m0 = rb * 64, n0 = cb * 64;

  if (wv >= 4) {
    int aoffs[4], boffs[4];
#pragma unroll
    for (int j = 0; j < 4; ++j) {
      const int r = 16 * w + 4 * j + (lane >> 4), pc = lane & 15;
      aoffs[j] = (m0 + r) * KT + ((pc ^ (r & 15)) * 8);
      const int rr = 32 * w + 8 * j + (lane >> 3), pb = lane & 7;
      const int f = ((rr >> 1) & 1) | (((rr >> 3) & 1) << 1);
      boffs[j] = rr * DM + n0 + (((pb >> 1) ^ f) * 16) + (pb & 1) * 8;
    }
    auto stage = [&](int s) {
      const unsigned dst = lds_addr(lds) + (s % NST) * STG;
      const int k0 = s * 128;
#pragma unroll
      for (int j = 0; j < 4; ++j) glds16(A + aoffs[j] + k0, dst + (4 * w + j) * 1024);
#pragma unroll
      for (int j = 0; j < 4; ++j) glds16(W + (size_t)k0 * DM + boffs[j], dst + 16384 + (4 * w + j) * 1024);
    };
#pragma unroll
    for (int s = 0; s < NST - 1; ++s) if (s < NSTG) stage(s);
#pragma unroll
    for (int i = 0; i < NSTG; ++i) {
      if (i + NST - 2 < NSTG) wait_vm<(NST - 2) * 8>();
      else if (NSTG - 1 - i == 0) wait_vm<0>();
      else wait_vm<8>();
      __builtin_amdgcn_s_barrier();
      asm volatile("" ::: "memory");
      if (i + NST - 1 < NSTG) stage(i + NST - 1);
    }
    __builtin_amdgcn_s_barrier();
    __builtin_amdgcn_s_barrier();
    return;
  }

  const size_t go = (size_t)(m0 + (tid >> 2)) * DM + n0 + (tid & 3) * 16;
  const u32x4 r0 = *(const u32x4*)(RES + go), r1 = *(const u32x4*)(RES + go + 8);
  const int fr = lane & 15, g = lane >> 4, q4 = (lane & 15) >> 2, p4 = lane & 3;
  f32x4 acc[4][4];
#pragma unroll
  for (int i = 0; i < 4; ++i)
#pragma unroll
    for (int j = 0; j < 4; ++j) acc[i][j] = (f32x4){0.f, 0.f, 0.f, 0.f};
  const int rB = 32 * w + 8 * g + q4;
  const int fB = ((rB >> 1) & 1) | (((rB >> 3) & 1) << 1);
  bf16x8 af[2][4], bfg[2][4];
  auto load_frags = [&](int s, int buf) {
    LDS_AS char* st = lds + (s % NST) * STG;
#pragma unroll
    for (int mt = 0; mt < 4; ++mt) af[buf][mt] = lds_read16(st + (16 * mt + fr) * 256 + (((4 * w + g) ^ fr) * 16));
#pragma unroll
    for (int nt = 0; nt < 4; ++nt) {
      const int o = 16384 + rB * 128 + ((nt ^ fB) * 32) + 8 * p4;
      const s16x4 lo = tr_read(st + o);
      const s16x4 hi = tr_read(st + o + 512);
      bfg[buf][nt] = __builtin_shufflevector(lo, hi, 0, 1, 2, 3, 4, 5, 6, 7);
    }
  };
#pragma unroll
  for (int i = 0; i <= NSTG; ++i) {
    wait_lgkm0();
    __builtin_amdgcn_s_barrier();
    asm volatile("" ::: "memory");
    if (i < NSTG) load_frags(i, i & 1);
    if (i > 0) {
#pragma unroll
      for (int mt = 0; mt < 4; ++mt)
#pragma unroll
        for (int nt = 0; nt < 4; ++nt)
          acc[mt][nt] = __builtin_amdgcn_mfma_f32_16x16x32_bf16(bfg[(i - 1) & 1][nt], af[(i - 1) & 1][mt], acc[mt][nt], 0, 0, 0);
      if (i < NSTG) {
#pragma unroll
        for (int q = 0; q < 12; ++q) {
          __builtin_amdgcn_sched_group_barrier(0x008, 1, 0);
          __builtin_amdgcn_sched_group_barrier(0x100, 1, 0);
        }
        __builtin_amdgcn_sched_group_barrier(0x008, 16, 0);
      }
    }
    __builtin_amdgcn_sched_barrier(0);
  }
  TS_MARK(0);
#pragma unroll
  for (int mt = 0; mt < 4; ++mt)
#pragma unroll
    for (int nt = 0; nt < 4; ++nt)
      *(LDS_AS f32x4*)(lds + w * 32768 + (16 * mt + fr) * 272 + (16 * nt + 4 * g) * 4) = acc[mt][nt];
  wait_lgkm0();
  __builtin_amdgcn_s_barrier();
  asm volatile("" ::: "memory");
  {
    const int row = tid >> 2, c0 = (tid & 3) * 16;
    float s[16];
#pragma unroll
    for (int e = 0; e < 16; ++e) s[e] = 0.f;
#pragma unroll
    for (int ww = 0; ww < 4; ++ww)
#pragma unroll
      for (int e4 = 0; e4 < 4; ++e4) {
        const f32x4 v = *(LDS_AS f32x4*)(lds + ww * 32768 + row * 272 + (c0 + 4 * e4) * 4);
        s[4 * e4] += v[0]; s[4 * e4 + 1] += v[1]; s[4 * e4 + 2] += v[2]; s[4 * e4 + 3] += v[3];
      }
    u32x4 o0, o1;
#pragma unroll
    for (int e = 0; e < 4; ++e) {
      o0[e] = pk2(bflo(r0[e]) + bfr(s[2 * e]), bfhi(r0[e]) + bfr(s[2 * e + 1]));
      o1[e] = pk2(bflo(r1[e]) + bfr(s[8 + 2 * e]), bfhi(r1[e]) + bfr(s[8 + 2 * e + 1]));
    }
    *(u32x4*)(OUT + go) = o0;
    *(u32x4*)(OUT + go + 8) = o1;
  }
  TS_MARK(1);
  TS_END(KT == 512 ? 2 : 4);
}

extern "C" void kernel_launch(void* const* d_in, const int* in_sizes, int n_in, void* d_out, int out_size,
                              void* d_ws, size_t ws_size, hipStream_t stream) {
  const u16* x = (const u16*)d_in[0];
  const u16* wqkv = (const u16*)d_in[1];
  const u16* wo = (const u16*)d_in[2];
  const u16* wg = (const u16*)d_in[4];
  const u16* wu = (const u16*)d_in[5];
  const u16* wd = (const u16*)d_in[6];
  u16* out = (u16*)d_out;
  char* ws = (char*)d_ws;
  const size_t MB2 = 2u << 20;
  u16* Qh = (u16*)(ws);
  u16* Kh = (u16*)(ws + MB2);
  u16* Vh = (u16*)(ws + 2 * MB2);
  u16* AO = (u16*)(ws + 3 * MB2);
  u16* Hb = (u16*)(ws + 4 * MB2);
  u16* Act = (u16*)(ws + 5 * MB2);

  ln_gemm_kernel<0><<<192, 512, 0, stream>>>(x, wqkv, wqkv, Qh, Kh, Vh);
  attn_kernel<<<256, 512, 0, stream>>>(Qh, Kh, Vh, AO);
  gemm_res_kernel<512><<<256, 512, 0, stream>>>(AO, wo, x, Hb);
  ln_gemm_kernel<1><<<224, 512, 0, stream>>>(Hb, wg, wu, Act, Act, Act);
  gemm_res_kernel<1792><<<256, 512, 0, stream>>>(Act, wd, Hb, out);
}
```

```cpp
#include <hip/hip_runtime.h>
#include <hip/hip_bf16.h>
#include <stdint.h>

typedef short bf16x8 __attribute__((ext_vector_type(8)));
typedef short s16x4 __attribute__((ext_vector_type(4)));
typedef float f32x2 __attribute__((ext_vector_type(2)));
typedef float f32x4 __attribute__((ext_vector_type(4)));
typedef float f32x16 __attribute__((ext_vector_type(16)));
typedef unsigned u32x4 __attribute__((ext_vector_type(4)));
typedef unsigned u32x2 __attribute__((ext_vector_type(2)));
typedef __bf16 bf16x2n __attribute__((ext_vector_type(2)));
typedef unsigned short u16;

#define LDS_AS __attribute__((address_space(3)))
#define DEVI __device__ __forceinline__

constexpr int SEQ = 2048, DM = 512, NH = 32, DH = 16, FF = 1792;
constexpr float QSCALE = 0.25f * 1.4426950408889634f;

DEVI unsigned pk2(float lo, float hi) {
  f32x2 v = {lo, hi};
  bf16x2n r = __builtin_convertvector(v, bf16x2n);
  return __builtin_bit_cast(unsigned, r);
}
DEVI float bflo(unsigned u) { return __uint_as_float(u << 16); }
DEVI float bfhi(unsigned u) { return __uint_as_float(u & 0xffff0000u); }
DEVI float bfr(float x) { return bflo(pk2(x, x)); }

template <int N> DEVI void wait_vm() { asm volatile("s_waitcnt vmcnt(%0)" ::"n"(N) : "memory"); }
DEVI void wait_lgkm0() { asm volatile("s_waitcnt lgkmcnt(0)" ::: "memory"); }

DEVI void glds16(const void* gsrc, unsigned lds_base) {
  unsigned keep;
  asm volatile("s_mov_b32 %0, m0\n\ts_mov_b32 m0, %2\n\ts_nop 0\n\tglobal_load_lds_dwordx4 %1, off\n\ts_mov_b32 m0, %0"
               : "=&s"(keep) : "v"(gsrc), "s"(lds_base) : "memory");
}
DEVI unsigned lds_addr(LDS_AS char* p) { return (unsigned)(uintptr_t)p; }
DEVI float quad_sum(float v) {
  v += __int_as_float(__builtin_amdgcn_mov_dpp(__float_as_int(v), 0xB1, 0xF, 0xF, true));
  v += __int_as_float(__builtin_amdgcn_mov_dpp(__float_as_int(v), 0x4E, 0xF, 0xF, true));
  return v;
}
DEVI s16x4 tr_read(LDS_AS char* p) { return __builtin_amdgcn_ds_read_tr16_b64_v4i16((LDS_AS s16x4*)p); }
DEVI bf16x8 lds_read16(LDS_AS char* p) { return *(LDS_AS bf16x8*)p; }

#define TS_DECL
#define TS_MARK(i)
#define TS_END(kid)

template <int MODE>
__global__ __launch_bounds__(512, 1) void ln_gemm_kernel(const u16* __restrict__ X, const u16* __restrict__ W0,
                                                        const u16* __restrict__ W1, u16* __restrict__ O0,
                                                        u16* __restrict__ O1, u16* __restrict__ O2) {
  constexpr int NW = (MODE == 0) ? 1536 : 1792;
  constexpr int NMAT = (MODE == 0) ? 1 : 2;
  constexpr int STG = 16384 * NMAT;
  constexpr int NST = (MODE == 0) ? 6 : 3;
  constexpr int NKS = 16;
  constexpr int L = 4 * NMAT;
  __shared__ __attribute__((aligned(16))) char smem[65536 + 98304];
  LDS_AS char* lds = (LDS_AS char*)smem;

  TS_DECL
  const int tid = threadIdx.x, lane = tid & 63, wv = __builtin_amdgcn_readfirstlane(tid >> 6);
  const int w = wv & 3;
  const int b = blockIdx.x;
  const int rb = (b & 7) * 4 + ((b >> 3) & 3), cb = b >> 5;
  const int m0 = rb * 64;
  const int n0 = cb * 256 + w * 64;
  LDS_AS char* ring = lds + 65536 + w * (4096 * NMAT);

  if (wv >= 4) {
    int srcoff[4];
#pragma unroll
    for (int j = 0; j < 4; ++j) {
      const int r = 8 * j + (lane >> 3), pc = lane & 7;
      const int f = ((r >> 1) & 1) | (((r >> 3) & 1) << 1);
      const int c = (pc >> 1) ^ f;
      srcoff[j] = r * NW + n0 + c * 16 + (pc & 1) * 8;
    }
    auto stage = [&](int ks) {
      const unsigned dst = lds_addr(ring) + (ks % NST) * STG;
#pragma unroll
      for (int mtx = 0; mtx < NMAT; ++mtx) {
        const u16* Wp = (mtx == 0 ? W0 : W1) + (size_t)ks * 32 * NW;
#pragma unroll
        for (int j = 0; j < 4; ++j) glds16(Wp + srcoff[j], dst + mtx * 4096 + j * 1024);
      }
    };
    __builtin_amdgcn_s_barrier();
    asm volatile("" ::: "memory");
#pragma unroll
    for (int s = 0; s < NST - 1; ++s) stage(s);
#pragma unroll
    for (int i = 0; i < NKS; ++i) {
      if (i + NST - 2 < NKS) wait_vm<(NST - 2) * L>();
      else if (NKS - 1 - i == 0) wait_vm<0>();
      else if (NKS - 1 - i == 1) wait_vm<1 * L>();
      else if (NKS - 1 - i == 2) wait_vm<2 * L>();
      else wait_vm<3 * L>();
      __builtin_amdgcn_s_barrier();
      asm volatile("" ::: "memory");
      if (i + NST - 1 < NKS) stage(i + NST - 1);
    }
    __builtin_amdgcn_s_barrier();
    return;
  }

  {
    const int row = w * 16 + (lane >> 2), part = lane & 3;
    const u16* xp = X + (size_t)(m0 + row) * DM + part * 8;
    u32x4 xr[16];
#pragma unroll
    for (int j = 0; j < 16; ++j) xr[j] = *(const u32x4*)(xp + j * 32);
    asm volatile("" ::: "memory");
    __builtin_amdgcn_s_barrier();
    asm volatile("" ::: "memory");
    const float x0 = __int_as_float(__builtin_amdgcn_mov_dpp((int)(xr[0][0] << 16), 0x00, 0xF, 0xF, true));
    const f32x2 piv = {x0, x0};
    f32x2 v[64];
    f32x2 sa = {0.f, 0.f}, sq = {0.f, 0.f};
#pragma unroll
    for (int j = 0; j < 16; ++j)
#pragma unroll
      for (int e = 0; e < 4; ++e) {
        f32x2 d = (f32x2){bflo(xr[j][e]), bfhi(xr[j][e])} - piv;
        v[4 * j + e] = d;
        sa += d;
        sq += d * d;
      }
    const float s1 = quad_sum(sa[0] + sa[1]) * (1.0f / DM);
    const float s2 = quad_sum(sq[0] + sq[1]) * (1.0f / DM);
    const float mean = x0 + s1;
    const float var = fmaxf(s2 - s1 * s1, 0.f);
    const float mb = bfr(mean);
    const float sd = bfr(sqrtf(bfr(bfr(var) + 1e-5f)));
    const float inv = 1.0f / sd;
    const f32x2 inv2 = {inv, inv}, c2 = {(x0 - mb) * inv, (x0 - mb) * inv};
#pragma unroll
    for (int j = 0; j < 16; ++j) {
      u32x4 y;
#pragma unroll
      for (int e = 0; e < 4; ++e) {
        const f32x2 r = v[4 * j + e] * inv2 + c2;
        y[e] = pk2(r[0], r[1]);
      }
      *(LDS_AS u32x4*)(lds + row * 1024 + (((part + 4 * j) ^ (row & 15)) * 16)) = y;
    }
  }
  TS_MARK(0);

  const int fr = lane & 15, g = lane >> 4, q4 = (lane & 15) >> 2, p4 = lane & 3;
  f32x4 acc[NMAT][4][4];
#pragma unroll
  for (int a = 0; a < NMAT; ++a)
#pragma unroll
    for (int i = 0; i < 4; ++i)
#pragma unroll
      for (int j = 0; j < 4; ++j) acc[a][i][j] = (f32x4){0.f, 0.f, 0.f, 0.f};
  const int rB = 8 * g + q4;
  const int fB = ((rB >> 1) & 1) | (((rB >> 3) & 1) << 1);
  int boff[4];
#pragma unroll
  for (int nt = 0; nt < 4; ++nt) boff[nt] = rB * 128 + ((nt ^ fB) * 32) + 8 * p4;
  const int aoff = fr * 1024;
  bf16x8 af[2][4], bfg[2][NMAT][4];
  auto load_frags = [&](int ks, int buf) {
    LDS_AS char* st = ring + (ks % NST) * STG;
#pragma unroll
    for (int mt = 0; mt < 4; ++mt) af[buf][mt] = lds_read16(lds + aoff + mt * 16384 + (((4 * ks + g) ^ fr) * 16));
#pragma unroll
    for (int mtx = 0; mtx < NMAT; ++mtx)
#pragma unroll
      for (int nt = 0; nt < 4; ++nt) {
        const s16x4 lo = tr_read(st + mtx * 4096 + boff[nt]);
        const s16x4 hi = tr_read(st + mtx * 4096 + boff[nt] + 512);
        bfg[buf][mtx][nt] = __builtin_shufflevector(lo, hi, 0, 1, 2, 3, 4, 5, 6, 7);
      }
  };
#pragma unroll
  for (int i = 0; i <= NKS; ++i) {
    wait_lgkm0();
    __builtin_amdgcn_s_barrier();
    asm volatile("" ::: "memory");

    if (i < NKS) load_frags(i, i & 1);
    if (i > 0) {
#pragma unroll
      for (int mtx = 0; mtx < NMAT; ++mtx)
#pragma unroll
        for (int mt = 0; mt < 4; ++mt)
#pragma unroll
          for (int nt = 0; nt < 4; ++nt)
            acc[mtx][mt][nt] = __builtin_amdgcn_mfma_f32_16x16x32_bf16(bfg[(i - 1) & 1][mtx][nt], af[(i - 1) & 1][mt], acc[mtx][mt][nt], 0, 0, 0);
      if (i < NKS) {
#pragma unroll
        for (int q = 0; q < 4 + 8 * NMAT; ++q) {
          __builtin_amdgcn_sched_group_barrier(0x008, 1, 0);
          __builtin_amdgcn_sched_group_barrier(0x100, 1, 0);
        }
        __builtin_amdgcn_sched_group_barrier(0x008, 16 * NMAT, 0);
      }
    }
    __builtin_amdgcn_sched_barrier(0);
  }
  TS_MARK(1);

#pragma unroll
  for (int mt = 0; mt < 4; ++mt) {
    const int tok = m0 + 16 * mt + fr;
#pragma unroll
    for (int nt = 0; nt < 4; ++nt) {
      const int col = n0 + 16 * nt + 4 * g;
      if constexpr (MODE == 0) {
        const int which = col >> 9, head = (col >> 4) & 31;
        f32x4 v = acc[0][mt][nt];
        u16* dst = (which == 0 ? O0 : (which == 1 ? O1 : O2));
        if (which == 0) v = v * QSCALE;
        u32x2 o = {pk2(v[0], v[1]), pk2(v[2], v[3])};
        *(u32x2*)(dst + ((size_t)head * SEQ + tok) * DH + 4 * g) = o;
      } else {
        const f32x4 gt = acc[0][mt][nt], up = acc[1][mt][nt];
        float r[4];
#pragma unroll
        for (int e = 0; e < 4; ++e) r[e] = fmaxf(gt[e], 0.f) * up[e];
        u32x2 o = {pk2(r[0], r[1]), pk2(r[2], r[3])};
        *(u32x2*)(O0 + (size_t)tok * FF + col) = o;
      }
    }
  }
  TS_MARK(2);
  TS_END(MODE == 0 ? 0 : 3);
}

constexpr int AT_K = 0, AT_V = 65536, AT_C = 131072;
__global__ __launch_bounds__(512, 1) void attn_kernel(const u16* __restrict__ Qh, const u16* __restrict__ Kh,
                                                      const u16* __restrict__ Vh, u16* __restrict__ AO,
                                                      const u16* __restrict__ PW0, const u16* __restrict__ PW1,
                                                      const u16* __restrict__ PW2, const u16* __restrict__ PW3) {
  __shared__ __attribute__((aligned(16))) char smem[131072 + 1024];
  LDS_AS char* lds = (LDS_AS char*)smem;
  const int tid = threadIdx.x, lane = tid & 63, w = __builtin_amdgcn_readfirstlane(tid >> 6);
  const int b = blockIdx.x;
  TS_DECL
  const int head = (b & 7) * 4 + (b >> 6), qb = (b >> 3) & 7;
  const u16* Kp = Kh + (size_t)head * SEQ * DH;
  const u16* Vp = Vh + (size_t)head * SEQ * DH;

  const int l31 = lane & 31, hh = lane >> 5;
  const int q0 = qb * 256 + w * 32;
  const bf16x8 qf = *(const bf16x8*)(Qh + ((size_t)head * SEQ + q0 + l31) * DH + 8 * hh);
#pragma unroll
  for (int j = 0; j < 8; ++j) {
    const int o = (w * 8 + j) * 1024 + lane * 16;
    const int key = o >> 5, piece = ((o >> 4) & 1) ^ ((key >> 3) & 1);
    glds16(Kp + key * 16 + piece * 8, lds_addr(lds) + AT_K + (w * 8 + j) * 1024);
  }
#pragma unroll
  for (int j = 0; j < 8; ++j) {
    const int o = (w * 8 + j) * 1024 + lane * 16;
    glds16(Vp + (o >> 1), lds_addr(lds) + AT_V + (w * 8 + j) * 1024);
  }
  unsigned pf = 0;
  if (w < 4) {
    const u16* pw = (w == 0) ? PW0 : (w == 1) ? PW1 : (w == 2) ? PW2 : PW3;
    const int nline = (w == 0) ? (DM * DM / 64) : (DM * FF / 64);
    const int line = b * 64 + lane;
    if (line < nline) pf = *(const unsigned*)(pw + (size_t)line * 64);
    if (line + 16384 < nline) pf += *(const unsigned*)(pw + (size_t)(line + 16384) * 64);
  }
  if (tid < 64) {
    const unsigned one = (lane & 1) ? 0u : 0x3f80u;
    u32x4 v = {one, 0u, 0u, 0u};
    *(LDS_AS u32x4*)(lds + AT_C + lane * 16) = v;
  }
  const int koff = AT_K + l31 * 32 + ((hh ^ ((l31 >> 3) & 1)) * 16);

  wait_vm<8>();
  wait_lgkm0();
  __builtin_amdgcn_s_barrier();
  asm volatile("" ::: "memory");

  TS_MARK(0);
  float mx = -3.0e38f;
  {
    const f32x16 zero = {};
#pragma unroll 4
    for (int t = 0; t < 64; ++t) {
      const bf16x8 kf = lds_read16(lds + koff + t * 1024);
      const f32x16 x = __builtin_amdgcn_mfma_f32_32x32x16_bf16(kf, qf, zero, 0, 0, 0);
#pragma unroll
      for (int r = 0; r < 16; r += 2) mx = fmaxf(fmaxf(mx, x[r]), x[r + 1]);
    }
    auto rr = __builtin_amdgcn_permlane32_swap(__float_as_uint(mx), __float_as_uint(mx), false, false);
    mx = fmaxf(__uint_as_float(rr[0]), __uint_as_float(rr[1]));
  }
  asm volatile("" :: "v"(pf));
  wait_vm<0>();
  __builtin_amdgcn_s_barrier();
  asm volatile("" ::: "memory");

  TS_MARK(1);
  f32x16 o = {};
  f32x16 cinit;
#pragma unroll
  for (int r = 0; r < 16; ++r) cinit[r] = -mx;
  const int up = (lane >> 4) & 1, q4 = (lane & 15) >> 2, p4 = lane & 3;
  int voff = (up ? (AT_C + 128 * (1 - hh)) : (AT_V + 128 * hh)) + q4 * 32 + 8 * p4;
  const int vinc = up ? 0 : 1024;
#pragma unroll 2
  for (int t = 0; t < 64; ++t) {
    const bf16x8 kf = lds_read16(lds + koff + t * 1024);
    const f32x16 x = __builtin_amdgcn_mfma_f32_32x32x16_bf16(kf, qf, cinit, 0, 0, 0);
    float pe[16];
#pragma unroll
    for (int r = 0; r < 16; ++r) pe[r] = __builtin_amdgcn_exp2f(x[r]);
#pragma unroll
    for (int s = 0; s < 2; ++s) {
      u32x4 pk;
#pragma unroll
      for (int e = 0; e < 4; ++e) pk[e] = pk2(pe[8 * s + 2 * e], pe[8 * s + 2 * e + 1]);
      const s16x4 lo = tr_read(lds + voff + s * 512);
      const s16x4 hi = tr_read(lds + voff + s * 512 + 256);
      const bf16x8 vf = __builtin_shufflevector(lo, hi, 0, 1, 2, 3, 4, 5, 6, 7);
      o = __builtin_amdgcn_mfma_f32_32x32x16_bf16(vf, __builtin_bit_cast(bf16x8, pk), o, 0, 0, 0);
    }
    voff += vinc;
  }
  TS_MARK(2);
  auto ls = __builtin_amdgcn_permlane32_swap(__float_as_uint(o[8]), __float_as_uint(o[8]), false, false);
  const float linv = 1.0f / __uint_as_float(ls[0]);
  u16* dst = AO + (size_t)(q0 + l31) * DM + head * DH + 4 * hh;
  u32x2 o0 = {pk2(o[0] * linv, o[1] * linv), pk2(o[2] * linv, o[3] * linv)};
  u32x2 o1 = {pk2(o[4] * linv, o[5] * linv), pk2(o[6] * linv, o[7] * linv)};
  *(u32x2*)(dst) = o0;
  *(u32x2*)(dst + 8) = o1;
  TS_MARK(3);
  TS_END(1);
}

template <int KT>
__global__ __launch_bounds__(512, 1) void gemm_res_kernel(const u16* __restrict__ A, const u16* __restrict__ W,
                                                         const u16* __restrict__ RES, u16* __restrict__ OUT) {
  constexpr int NSTG = KT / 128;
  constexpr int NST = 4;
  constexpr int STG = 32768;
  __shared__ __attribute__((aligned(16))) char smem[NST * STG];
  LDS_AS char* lds = (LDS_AS char*)smem;
  TS_DECL
  const int tid = threadIdx.x, lane = tid & 63, wv = __builtin_amdgcn_readfirstlane(tid >> 6);
  const int w = wv & 3;
  const int b = blockIdx.x;
  const int rb = (b & 7) * 4 + ((b >> 3) & 3), cb = b >> 5;
  const int m0 = rb * 64, n0 = cb * 64;

  if (wv >= 4) {
    int aoffs[4], boffs[4];
#pragma unroll
    for (int j = 0; j < 4; ++j) {
      const int r = 16 * w + 4 * j + (lane >> 4), pc = lane & 15;
      aoffs[j] = (m0 + r) * KT + ((pc ^ (r & 15)) * 8);
      const int rr = 32 * w + 8 * j + (lane >> 3), pb = lane & 7;
      const int f = ((rr >> 1) & 1) | (((rr >> 3) & 1) << 1);
      boffs[j] = rr * DM + n0 + (((pb >> 1) ^ f) * 16) + (pb & 1) * 8;
    }
    auto stage = [&](int s) {
      const unsigned dst = lds_addr(lds) + (s % NST) * STG;
      const int k0 = s * 128;
#pragma unroll
      for (int j = 0; j < 4; ++j) glds16(A + aoffs[j] + k0, dst + (4 * w + j) * 1024);
#pragma unroll
      for (int j = 0; j < 4; ++j) glds16(W + (size_t)k0 * DM + boffs[j], dst + 16384 + (4 * w + j) * 1024);
    };
#pragma unroll
    for (int s = 0; s < NST - 1; ++s) if (s < NSTG) stage(s);
#pragma unroll
    for (int i = 0; i < NSTG; ++i) {
      if (i + NST - 2 < NSTG) wait_vm<(NST - 2) * 8>();
      else if (NSTG - 1 - i == 0) wait_vm<0>();
      else wait_vm<8>();
      __builtin_amdgcn_s_barrier();
      asm volatile("" ::: "memory");
      if (i + NST - 1 < NSTG) stage(i + NST - 1);
    }
    __builtin_amdgcn_s_barrier();
    __builtin_amdgcn_s_barrier();
    return;
  }

  const size_t go = (size_t)(m0 + (tid >> 2)) * DM + n0 + (tid & 3) * 16;
  const u32x4 r0 = *(const u32x4*)(RES + go), r1 = *(const u32x4*)(RES + go + 8);
  const int fr = lane & 15, g = lane >> 4, q4 = (lane & 15) >> 2, p4 = lane & 3;
  f32x4 acc[4][4];
#pragma unroll
  for (int i = 0; i < 4; ++i)
#pragma unroll
    for (int j = 0; j < 4; ++j) acc[i][j] = (f32x4){0.f, 0.f, 0.f, 0.f};
  const int rB = 32 * w + 8 * g + q4;
  const int fB = ((rB >> 1) & 1) | (((rB >> 3) & 1) << 1);
  bf16x8 af[2][4], bfg[2][4];
  auto load_frags = [&](int s, int buf) {
    LDS_AS char* st = lds + (s % NST) * STG;
#pragma unroll
    for (int mt = 0; mt < 4; ++mt) af[buf][mt] = lds_read16(st + (16 * mt + fr) * 256 + (((4 * w + g) ^ fr) * 16));
#pragma unroll
    for (int nt = 0; nt < 4; ++nt) {
      const int o = 16384 + rB * 128 + ((nt ^ fB) * 32) + 8 * p4;
      const s16x4 lo = tr_read(st + o);
      const s16x4 hi = tr_read(st + o + 512);
      bfg[buf][nt] = __builtin_shufflevector(lo, hi, 0, 1, 2, 3, 4, 5, 6, 7);
    }
  };
#pragma unroll
  for (int i = 0; i <= NSTG; ++i) {
    wait_lgkm0();
    __builtin_amdgcn_s_barrier();
    asm volatile("" ::: "memory");
    if (i < NSTG) load_frags(i, i & 1);
    if (i > 0) {
#pragma unroll
      for (int mt = 0; mt < 4; ++mt)
#pragma unroll
        for (int nt = 0; nt < 4; ++nt)
          acc[mt][nt] = __builtin_amdgcn_mfma_f32_16x16x32_bf16(bfg[(i - 1) & 1][nt], af[(i - 1) & 1][mt], acc[mt][nt], 0, 0, 0);
      if (i < NSTG) {
#pragma unroll
        for (int q = 0; q < 12; ++q) {
          __builtin_amdgcn_sched_group_barrier(0x008, 1, 0);
          __builtin_amdgcn_sched_group_barrier(0x100, 1, 0);
        }
        __builtin_amdgcn_sched_group_barrier(0x008, 16, 0);
      }
    }
    __builtin_amdgcn_sched_barrier(0);
  }
  TS_MARK(0);
#pragma unroll
  for (int mt = 0; mt < 4; ++mt)
#pragma unroll
    for (int nt = 0; nt < 4; ++nt)
      *(LDS_AS f32x4*)(lds + w * 32768 + (16 * mt + fr) * 272 + (16 * nt + 4 * g) * 4) = acc[mt][nt];
  wait_lgkm0();
  __builtin_amdgcn_s_barrier();
  asm volatile("" ::: "memory");
  {
    const int row = tid >> 2, c0 = (tid & 3) * 16;
    float s[16];
#pragma unroll
    for (int e = 0; e < 16; ++e) s[e] = 0.f;
#pragma unroll
    for (int ww = 0; ww < 4; ++ww)
#pragma unroll
      for (int e4 = 0; e4 < 4; ++e4) {
        const f32x4 v = *(LDS_AS f32x4*)(lds + ww * 32768 + row * 272 + (c0 + 4 * e4) * 4);
        s[4 * e4] += v[0]; s[4 * e4 + 1] += v[1]; s[4 * e4 + 2] += v[2]; s[4 * e4 + 3] += v[3];
      }
    u32x4 o0, o1;
#pragma unroll
    for (int e = 0; e < 4; ++e) {
      o0[e] = pk2(bflo(r0[e]) + bfr(s[2 * e]), bfhi(r0[e]) + bfr(s[2 * e + 1]));
      o1[e] = pk2(bflo(r1[e]) + bfr(s[8 + 2 * e]), bfhi(r1[e]) + bfr(s[8 + 2 * e + 1]));
    }
    *(u32x4*)(OUT + go) = o0;
    *(u32x4*)(OUT + go + 8) = o1;
  }
  TS_MARK(1);
  TS_END(KT == 512 ? 2 : 4);
}

extern "C" void kernel_launch(void* const* d_in, const int* in_sizes, int n_in, void* d_out, int out_size,
                              void* d_ws, size_t ws_size, hipStream_t stream) {
  const u16* x = (const u16*)d_in[0];
  const u16* wqkv = (const u16*)d_in[1];
  const u16* wo = (const u16*)d_in[2];
  const u16* wg = (const u16*)d_in[4];
  const u16* wu = (const u16*)d_in[5];
  const u16* wd = (const u16*)d_in[6];
  u16* out = (u16*)d_out;
  char* ws = (char*)d_ws;
  const size_t MB2 = 2u << 20;
  u16* Qh = (u16*)(ws);
  u16* Kh = (u16*)(ws + MB2);
  u16* Vh = (u16*)(ws + 2 * MB2);
  u16* AO = (u16*)(ws + 3 * MB2);
  u16* Hb = (u16*)(ws + 4 * MB2);
  u16* Act = (u16*)(ws + 5 * MB2);

  ln_gemm_kernel<0><<<192, 512, 0, stream>>>(x, wqkv, wqkv, Qh, Kh, Vh);
  attn_kernel<<<256, 512, 0, stream>>>(Qh, Kh, Vh, AO, wo, wg, wu, wd);
  gemm_res_kernel<512><<<256, 512, 0, stream>>>(AO, wo, x, Hb);
  ln_gemm_kernel<1><<<224, 512, 0, stream>>>(Hb, wg, wu, Act, Act, Act);
  gemm_res_kernel<1792><<<256, 512, 0, stream>>>(Act, wd, Hb, out);
}
```

```cpp
#include <hip/hip_runtime.h>
#include <hip/hip_bf16.h>
#include <stdint.h>

typedef short bf16x8 __attribute__((ext_vector_type(8)));
typedef short s16x4 __attribute__((ext_vector_type(4)));
typedef float f32x2 __attribute__((ext_vector_type(2)));
typedef float f32x4 __attribute__((ext_vector_type(4)));
typedef float f32x16 __attribute__((ext_vector_type(16)));
typedef unsigned u32x4 __attribute__((ext_vector_type(4)));
typedef unsigned u32x2 __attribute__((ext_vector_type(2)));
typedef __bf16 bf16x2n __attribute__((ext_vector_type(2)));
typedef unsigned short u16;

#define LDS_AS __attribute__((address_space(3)))
#define DEVI __device__ __forceinline__

constexpr int SEQ = 2048, DM = 512, NH = 32, DH = 16, FF = 1792;
constexpr float QSCALE = 0.25f * 1.4426950408889634f;

DEVI unsigned pk2(float lo, float hi) {
  f32x2 v = {lo, hi};
  bf16x2n r = __builtin_convertvector(v, bf16x2n);
  return __builtin_bit_cast(unsigned, r);
}
DEVI float bflo(unsigned u) { return __uint_as_float(u << 16); }
DEVI float bfhi(unsigned u) { return __uint_as_float(u & 0xffff0000u); }
DEVI float bfr(float x) { return bflo(pk2(x, x)); }

template <int N> DEVI void wait_vm() { asm volatile("s_waitcnt vmcnt(%0)" ::"n"(N) : "memory"); }
DEVI void wait_lgkm0() { asm volatile("s_waitcnt lgkmcnt(0)" ::: "memory"); }

DEVI void glds16(const void* gsrc, unsigned lds_base) {
  unsigned keep;
  asm volatile("s_mov_b32 %0, m0\n\ts_mov_b32 m0, %2\n\ts_nop 0\n\tglobal_load_lds_dwordx4 %1, off\n\ts_mov_b32 m0, %0"
               : "=&s"(keep) : "v"(gsrc), "s"(lds_base) : "memory");
}
DEVI unsigned lds_addr(LDS_AS char* p) { return (unsigned)(uintptr_t)p; }
DEVI float quad_sum(float v) {
  v += __int_as_float(__builtin_amdgcn_mov_dpp(__float_as_int(v), 0xB1, 0xF, 0xF, true));
  v += __int_as_float(__builtin_amdgcn_mov_dpp(__float_as_int(v), 0x4E, 0xF, 0xF, true));
  return v;
}
DEVI s16x4 tr_read(LDS_AS char* p) { return __builtin_amdgcn_ds_read_tr16_b64_v4i16((LDS_AS s16x4*)p); }
DEVI bf16x8 lds_read16(LDS_AS char* p) { return *(LDS_AS bf16x8*)p; }

#define TS_DECL
#define TS_MARK(i)
#define TS_END(kid)

template <int MODE>
__global__ __launch_bounds__(512, 1) void ln_gemm_kernel(const u16* __restrict__ X, const u16* __restrict__ W0,
                                                        const u16* __restrict__ W1, u16* __restrict__ O0,
                                                        u16* __restrict__ O1, u16* __restrict__ O2) {
  constexpr int NW = (MODE == 0) ? 1536 : 1792;
  constexpr int NMAT = (MODE == 0) ? 1 : 2;
  constexpr int STG = 16384 * NMAT;
  constexpr int NST = (MODE == 0) ? 6 : 3;
  constexpr int NKS = 16;
  constexpr int L = 4 * NMAT;
  __shared__ __attribute__((aligned(16))) char smem[65536 + 98304];
  LDS_AS char* lds = (LDS_AS char*)smem;

  TS_DECL
  const int tid = threadIdx.x, lane = tid & 63, wv = __builtin_amdgcn_readfirstlane(tid >> 6);
  const int w = wv & 3;
  const int b = blockIdx.x;
  const int rb = (b & 7) * 4 + ((b >> 3) & 3), cb = b >> 5;
  const int m0 = rb * 64;
  const int n0 = cb * 256 + w * 64;
  LDS_AS char* ring = lds + 65536 + w * (4096 * NMAT);

  if (wv >= 4) {
    int srcoff[4];
#pragma unroll
    for (int j = 0; j < 4; ++j) {
      const int r = 8 * j + (lane >> 3), pc = lane & 7;
      const int f = ((r >> 1) & 1) | (((r >> 3) & 1) << 1);
      const int c = (pc >> 1) ^ f;
      srcoff[j] = r * NW + n0 + c * 16 + (pc & 1) * 8;
    }
    auto stage = [&](int ks) {
      const unsigned dst = lds_addr(ring) + (ks % NST) * STG;
#pragma unroll
      for (int mtx = 0; mtx < NMAT; ++mtx) {
        const u16* Wp = (mtx == 0 ? W0 : W1) + (size_t)ks * 32 * NW;
#pragma unroll
        for (int j = 0; j < 4; ++j) glds16(Wp + srcoff[j], dst + mtx * 4096 + j * 1024);
      }
    };
    __builtin_amdgcn_s_barrier();
    asm volatile("" ::: "memory");
#pragma unroll
    for (int s = 0; s < NST - 1; ++s) stage(s);
#pragma unroll
    for (int i = 0; i < NKS; ++i) {
      if (i + NST - 2 < NKS) wait_vm<(NST - 2) * L>();
      else if (NKS - 1 - i == 0) wait_vm<0>();
      else if (NKS - 1 - i == 1) wait_vm<1 * L>();
      else if (NKS - 1 - i == 2) wait_vm<2 * L>();
      else wait_vm<3 * L>();
      __builtin_amdgcn_s_barrier();
      asm volatile("" ::: "memory");
      if (i + NST - 1 < NKS) stage(i + NST - 1);
    }
    __builtin_amdgcn_s_barrier();
    return;
  }

  {
    const int row = w * 16 + (lane >> 2), part = lane & 3;
    const u16* xp = X + (size_t)(m0 + row) * DM + part * 8;
    u32x4 xr[16];
#pragma unroll
    for (int j = 0; j < 16; ++j) xr[j] = *(const u32x4*)(xp + j * 32);
    asm volatile("" ::: "memory");
    __builtin_amdgcn_s_barrier();
    asm volatile("" ::: "memory");
    const float x0 = __int_as_float(__builtin_amdgcn_mov_dpp((int)(xr[0][0] << 16), 0x00, 0xF, 0xF, true));
    const f32x2 piv = {x0, x0};
    f32x2 v[64];
    f32x2 sa = {0.f, 0.f}, sq = {0.f, 0.f};
#pragma unroll
    for (int j = 0; j < 16; ++j)
#pragma unroll
      for (int e = 0; e < 4; ++e) {
        f32x2 d = (f32x2){bflo(xr[j][e]), bfhi(xr[j][e])} - piv;
        v[4 * j + e] = d;
        sa += d;
        sq += d * d;
      }
    const float s1 = quad_sum(sa[0] + sa[1]) * (1.0f / DM);
    const float s2 = quad_sum(sq[0] + sq[1]) * (1.0f / DM);
    const float mean = x0 + s1;
    const float var = fmaxf(s2 - s1 * s1, 0.f);
    const float mb = bfr(mean);
    const float sd = bfr(sqrtf(bfr(bfr(var) + 1e-5f)));
    const float inv = 1.0f / sd;
    const f32x2 inv2 = {inv, inv}, c2 = {(x0 - mb) * inv, (x0 - mb) * inv};
#pragma unroll
    for (int j = 0; j < 16; ++j) {
      u32x4 y;
#pragma unroll
      for (int e = 0; e < 4; ++e) {
        const f32x2 r = v[4 * j + e] * inv2 + c2;
        y[e] = pk2(r[0], r[1]);
      }
      *(LDS_AS u32x4*)(lds + row * 1024 + (((part + 4 * j) ^ (row & 15)) * 16)) = y;
    }
  }
  TS_MARK(0);

  const int fr = lane & 15, g = lane >> 4, q4 = (lane & 15) >> 2, p4 = lane & 3;
  f32x4 acc[NMAT][4][4];
#pragma unroll
  for (int a = 0; a < NMAT; ++a)
#pragma unroll
    for (int i = 0; i < 4; ++i)
#pragma unroll
      for (int j = 0; j < 4; ++j) acc[a][i][j] = (f32x4){0.f, 0.f, 0.f, 0.f};
  const int rB = 8 * g + q4;
  const int fB = ((rB >> 1) & 1) | (((rB >> 3) & 1) << 1);
  int boff[4];
#pragma unroll
  for (int nt = 0; nt < 4; ++nt) boff[nt] = rB * 128 + ((nt ^ fB) * 32) + 8 * p4;
  const int aoff = fr * 1024;
  bf16x8 af[2][4], bfg[2][NMAT][4];
  auto load_frags = [&](int ks, int buf) {
    LDS_AS char* st = ring + (ks % NST) * STG;
#pragma unroll
    for (int mt = 0; mt < 4; ++mt) af[buf][mt] = lds_read16(lds + aoff + mt * 16384 + (((4 * ks + g) ^ fr) * 16));
#pragma unroll
    for (int mtx = 0; mtx < NMAT; ++mtx)
#pragma unroll
      for (int nt = 0; nt < 4; ++nt) {
        const s16x4 lo = tr_read(st + mtx * 4096 + boff[nt]);
        const s16x4 hi = tr_read(st + mtx * 4096 + boff[nt] + 512);
        bfg[buf][mtx][nt] = __builtin_shufflevector(lo, hi, 0, 1, 2, 3, 4, 5, 6, 7);
      }
  };
#pragma unroll
  for (int i = 0; i <= NKS; ++i) {
    wait_lgkm0();
    __builtin_amdgcn_s_barrier();
    asm volatile("" ::: "memory");

    if (i < NKS) load_frags(i, i & 1);
    if (i > 0) {
#pragma unroll
      for (int mtx = 0; mtx < NMAT; ++mtx)
#pragma unroll
        for (int mt = 0; mt < 4; ++mt)
#pragma unroll
          for (int nt = 0; nt < 4; ++nt)
            acc[mtx][mt][nt] = __builtin_amdgcn_mfma_f32_16x16x32_bf16(bfg[(i - 1) & 1][mtx][nt], af[(i - 1) & 1][mt], acc[mtx][mt][nt], 0, 0, 0);
      if (i < NKS) {
#pragma unroll
        for (int q = 0; q < 4 + 8 * NMAT; ++q) {
          __builtin_amdgcn_sched_group_barrier(0x008, 1, 0);
          __builtin_amdgcn_sched_group_barrier(0x100, 1, 0);
        }
        __builtin_amdgcn_sched_group_barrier(0x008, 16 * NMAT, 0);
      }
    }
    __builtin_amdgcn_sched_barrier(0);
  }
  TS_MARK(1);

#pragma unroll
  for (int mt = 0; mt < 4; ++mt) {
    const int tok = m0 + 16 * mt + fr;
#pragma unroll
    for (int nt = 0; nt < 4; ++nt) {
      const int col = n0 + 16 * nt + 4 * g;
      if constexpr (MODE == 0) {
        const int which = col >> 9, head = (col >> 4) & 31;
        f32x4 v = acc[0][mt][nt];
        u16* dst = (which == 0 ? O0 : (which == 1 ? O1 : O2));
        if (which == 0) v = v * QSCALE;
        u32x2 o = {pk2(v[0], v[1]), pk2(v[2], v[3])};
        *(u32x2*)(dst + ((size_t)head * SEQ + tok) * DH + 4 * g) = o;
      } else {
        const f32x4 gt = acc[0][mt][nt], up = acc[1][mt][nt];
        float r[4];
#pragma unroll
        for (int e = 0; e < 4; ++e) r[e] = fmaxf(gt[e], 0.f) * up[e];
        u32x2 o = {pk2(r[0], r[1]), pk2(r[2], r[3])};
        *(u32x2*)(O0 + (size_t)tok * FF + col) = o;
      }
    }
  }
  TS_MARK(2);
  TS_END(MODE == 0 ? 0 : 3);
}

constexpr int AT_K = 0, AT_V = 65536, AT_C = 131072, AT_X = 132096;
__global__ __launch_bounds__(1024, 1) void attn_kernel(const u16* __restrict__ Qh, const u16* __restrict__ Kh,
                                                       const u16* __restrict__ Vh, u16* __restrict__ AO,
                                                       const u16* __restrict__ PW0, const u16* __restrict__ PW1,
                                                       const u16* __restrict__ PW2, const u16* __restrict__ PW3) {
  __shared__ __attribute__((aligned(16))) char smem[132096 + 4096 + 8 * 64 * 9 * 4];
  LDS_AS char* lds = (LDS_AS char*)smem;
  const int tid = threadIdx.x, lane = tid & 63, wv = __builtin_amdgcn_readfirstlane(tid >> 6);
  const int w = wv & 7, kh = wv >> 3;
  const int b = blockIdx.x;
  TS_DECL
  const int head = (b & 7) * 4 + (b >> 6), qb = (b >> 3) & 7;
  const u16* Kp = Kh + (size_t)head * SEQ * DH;
  const u16* Vp = Vh + (size_t)head * SEQ * DH;
  const int l31 = lane & 31, hh = lane >> 5;
  const int q0 = qb * 256 + w * 32;
  const bf16x8 qf = *(const bf16x8*)(Qh + ((size_t)head * SEQ + q0 + l31) * DH + 8 * hh);
#pragma unroll
  for (int j = 0; j < 4; ++j) {
    const int o = (wv * 4 + j) * 1024 + lane * 16;
    const int key = o >> 5, piece = ((o >> 4) & 1) ^ ((key >> 3) & 1);
    glds16(Kp + key * 16 + piece * 8, lds_addr(lds) + AT_K + (wv * 4 + j) * 1024);
  }
#pragma unroll
  for (int j = 0; j < 4; ++j) {
    const int o = (wv * 4 + j) * 1024 + lane * 16;
    glds16(Vp + (o >> 1), lds_addr(lds) + AT_V + (wv * 4 + j) * 1024);
  }
  unsigned pf = 0;
  if (wv < 4) {
    const u16* pw = (wv == 0) ? PW0 : (wv == 1) ? PW1 : (wv == 2) ? PW2 : PW3;
    const int nline = (wv == 0) ? (DM * DM / 64) : (DM * FF / 64);
    const int line = b * 64 + lane;
    if (line < nline) pf = *(const unsigned*)(pw + (size_t)line * 64);
  }
  if (tid < 64) {
    const unsigned one = (lane & 1) ? 0u : 0x3f80u;
    u32x4 v = {one, 0u, 0u, 0u};
    *(LDS_AS u32x4*)(lds + AT_C + lane * 16) = v;
  }
  const int koff = AT_K + kh * 32768 + l31 * 32 + ((hh ^ ((l31 >> 3) & 1)) * 16);

  wait_vm<4>();
  wait_lgkm0();
  __builtin_amdgcn_s_barrier();
  asm volatile("" ::: "memory");

  TS_MARK(0);
  float mx = -3.0e38f;
  {
    const f32x16 zero = {};
#pragma unroll 4
    for (int t = 0; t < 32; ++t) {
      const bf16x8 kf = lds_read16(lds + koff + t * 1024);
      const f32x16 x = __builtin_amdgcn_mfma_f32_32x32x16_bf16(kf, qf, zero, 0, 0, 0);
#pragma unroll
      for (int r = 0; r < 16; r += 2) mx = fmaxf(fmaxf(mx, x[r]), x[r + 1]);
    }
    auto rr = __builtin_amdgcn_permlane32_swap(__float_as_uint(mx), __float_as_uint(mx), false, false);
    mx = fmaxf(__uint_as_float(rr[0]), __uint_as_float(rr[1]));
  }
  *(LDS_AS float*)(lds + AT_X + (wv * 64 + lane) * 4) = mx;
  asm volatile("" :: "v"(pf));
  wait_vm<0>();
  wait_lgkm0();
  __builtin_amdgcn_s_barrier();
  asm volatile("" ::: "memory");
  mx = fmaxf(mx, *(LDS_AS float*)(lds + AT_X + ((wv ^ 8) * 64 + lane) * 4));

  TS_MARK(1);
  f32x16 o = {};
  f32x16 cinit;
#pragma unroll
  for (int r = 0; r < 16; ++r) cinit[r] = -mx;
  const int up = (lane >> 4) & 1, q4 = (lane & 15) >> 2, p4 = lane & 3;
  int voff = (up ? (AT_C + 128 * (1 - hh)) : (AT_V + kh * 32768 + 128 * hh)) + q4 * 32 + 8 * p4;
  const int vinc = up ? 0 : 1024;
#pragma unroll 2
  for (int t = 0; t < 32; ++t) {
    const bf16x8 kf = lds_read16(lds + koff + t * 1024);
    const f32x16 x = __builtin_amdgcn_mfma_f32_32x32x16_bf16(kf, qf, cinit, 0, 0, 0);
    float pe[16];
#pragma unroll
    for (int r = 0; r < 16; ++r) pe[r] = __builtin_amdgcn_exp2f(x[r]);
#pragma unroll
    for (int s = 0; s < 2; ++s) {
      u32x4 pk;
#pragma unroll
      for (int e = 0; e < 4; ++e) pk[e] = pk2(pe[8 * s + 2 * e], pe[8 * s + 2 * e + 1]);
      const s16x4 lo = tr_read(lds + voff + s * 512);
      const s16x4 hi = tr_read(lds + voff + s * 512 + 256);
      const bf16x8 vf = __builtin_shufflevector(lo, hi, 0, 1, 2, 3, 4, 5, 6, 7);
      o = __builtin_amdgcn_mfma_f32_32x32x16_bf16(vf, __builtin_bit_cast(bf16x8, pk), o, 0, 0, 0);
    }
    voff += vinc;
  }
  TS_MARK(2);
  LDS_AS float* xo = (LDS_AS float*)(lds + AT_X + 4096) + (w * 64 + lane) * 9;
  if (kh == 1) {
#pragma unroll
    for (int r = 0; r < 9; ++r) xo[r] = o[r];
  }
  wait_lgkm0();
  __builtin_amdgcn_s_barrier();
  asm volatile("" ::: "memory");
  if (kh == 0) {
#pragma unroll
    for (int r = 0; r < 9; ++r) o[r] += xo[r];
    auto ls = __builtin_amdgcn_permlane32_swap(__float_as_uint(o[8]), __float_as_uint(o[8]), false, false);
    const float linv = 1.0f / __uint_as_float(ls[0]);
    u16* dst = AO + (size_t)(q0 + l31) * DM + head * DH + 4 * hh;
    u32x2 o0 = {pk2(o[0] * linv, o[1] * linv), pk2(o[2] * linv, o[3] * linv)};
    u32x2 o1 = {pk2(o[4] * linv, o[5] * linv), pk2(o[6] * linv, o[7] * linv)};
    *(u32x2*)(dst) = o0;
    *(u32x2*)(dst + 8) = o1;
  }
  TS_MARK(3);
  TS_END(1);
}

template <int KT>
__global__ __launch_bounds__(512, 1) void gemm_res_kernel(const u16* __restrict__ A, const u16* __restrict__ W,
                                                         const u16* __restrict__ RES, u16* __restrict__ OUT) {
  constexpr int NSTG = KT / 128;
  constexpr int NST = 4;
  constexpr int STG = 32768;
  __shared__ __attribute__((aligned(16))) char smem[NST * STG];
  LDS_AS char* lds = (LDS_AS char*)smem;
  TS_DECL
  const int tid = threadIdx.x, lane = tid & 63, wv = __builtin_amdgcn_readfirstlane(tid >> 6);
  const int w = wv & 3;
  const int b = blockIdx.x;
  const int rb = (b & 7) * 4 + ((b >> 3) & 3), cb = b >> 5;
  const int m0 = rb * 64, n0 = cb * 64;

  if (wv >= 4) {
    int aoffs[4], boffs[4];
#pragma unroll
    for (int j = 0; j < 4; ++j) {
      const int r = 16 * w + 4 * j + (lane >> 4), pc = lane & 15;
      aoffs[j] = (m0 + r) * KT + ((pc ^ (r & 15)) * 8);
      const int rr = 32 * w + 8 * j + (lane >> 3), pb = lane & 7;
      const int f = ((rr >> 1) & 1) | (((rr >> 3) & 1) << 1);
      boffs[j] = rr * DM + n0 + (((pb >> 1) ^ f) * 16) + (pb & 1) * 8;
    }
    auto stage = [&](int s) {
      const unsigned dst = lds_addr(lds) + (s % NST) * STG;
      const int k0 = s * 128;
#pragma unroll
      for (int j = 0; j < 4; ++j) glds16(A + aoffs[j] + k0, dst + (4 * w + j) * 1024);
#pragma unroll
      for (int j = 0; j < 4; ++j) glds16(W + (size_t)k0 * DM + boffs[j], dst + 16384 + (4 * w + j) * 1024);
    };
#pragma unroll
    for (int s = 0; s < NST - 1; ++s) if (s < NSTG) stage(s);
#pragma unroll
    for (int i = 0; i < NSTG; ++i) {
      if (i + NST - 2 < NSTG) wait_vm<(NST - 2) * 8>();
      else if (NSTG - 1 - i == 0) wait_vm<0>();
      else wait_vm<8>();
      __builtin_amdgcn_s_barrier();
      asm volatile("" ::: "memory");
      if (i + NST - 1 < NSTG) stage(i + NST - 1);
    }
    __builtin_amdgcn_s_barrier();
    __builtin_amdgcn_s_barrier();
    return;
  }

  const size_t go = (size_t)(m0 + (tid >> 2)) * DM + n0 + (tid & 3) * 16;
  const u32x4 r0 = *(const u32x4*)(RES + go), r1 = *(const u32x4*)(RES + go + 8);
  const int fr = lane & 15, g = lane >> 4, q4 = (lane & 15) >> 2, p4 = lane & 3;
  f32x4 acc[4][4];
#pragma unroll
  for (int i = 0; i < 4; ++i)
#pragma unroll
    for (int j = 0; j < 4; ++j) acc[i][j] = (f32x4){0.f, 0.f, 0.f, 0.f};
  const int rB = 32 * w + 8 * g + q4;
  const int fB = ((rB >> 1) & 1) | (((rB >> 3) & 1) << 1);
  bf16x8 af[2][4], bfg[2][4];
  auto load_frags = [&](int s, int buf) {
    LDS_AS char* st = lds + (s % NST) * STG;
#pragma unroll
    for (int mt = 0; mt < 4; ++mt) af[buf][mt] = lds_read16(st + (16 * mt + fr) * 256 + (((4 * w + g) ^ fr) * 16));
#pragma unroll
    for (int nt = 0; nt < 4; ++nt) {
      const int o = 16384 + rB * 128 + ((nt ^ fB) * 32) + 8 * p4;
      const s16x4 lo = tr_read(st + o);
      const s16x4 hi = tr_read(st + o + 512);
      bfg[buf][nt] = __builtin_shufflevector(lo, hi, 0, 1, 2, 3, 4, 5, 6, 7);
    }
  };
#pragma unroll
  for (int i = 0; i <= NSTG; ++i) {
    wait_lgkm0();
    __builtin_amdgcn_s_barrier();
    asm volatile("" ::: "memory");
    if (i < NSTG) load_frags(i, i & 1);
    if (i > 0) {
#pragma unroll
      for (int mt = 0; mt < 4; ++mt)
#pragma unroll
        for (int nt = 0; nt < 4; ++nt)
          acc[mt][nt] = __builtin_amdgcn_mfma_f32_16x16x32_bf16(bfg[(i - 1) & 1][nt], af[(i - 1) & 1][mt], acc[mt][nt], 0, 0, 0);
      if (i < NSTG) {
#pragma unroll
        for (int q = 0; q < 12; ++q) {
          __builtin_amdgcn_sched_group_barrier(0x008, 1, 0);
          __builtin_amdgcn_sched_group_barrier(0x100, 1, 0);
        }
        __builtin_amdgcn_sched_group_barrier(0x008, 16, 0);
      }
    }
    __builtin_amdgcn_sched_barrier(0);
  }
  TS_MARK(0);
#pragma unroll
  for (int mt = 0; mt < 4; ++mt)
#pragma unroll
    for (int nt = 0; nt < 4; ++nt)
      *(LDS_AS f32x4*)(lds + w * 32768 + (16 * mt + fr) * 272 + (16 * nt + 4 * g) * 4) = acc[mt][nt];
  wait_lgkm0();
  __builtin_amdgcn_s_barrier();
  asm volatile("" ::: "memory");
  {
    const int row = tid >> 2, c0 = (tid & 3) * 16;
    float s[16];
#pragma unroll
    for (int e = 0; e < 16; ++e) s[e] = 0.f;
#pragma unroll
    for (int ww = 0; ww < 4; ++ww)
#pragma unroll
      for (int e4 = 0; e4 < 4; ++e4) {
        const f32x4 v = *(LDS_AS f32x4*)(lds + ww * 32768 + row * 272 + (c0 + 4 * e4) * 4);
        s[4 * e4] += v[0]; s[4 * e4 + 1] += v[1]; s[4 * e4 + 2] += v[2]; s[4 * e4 + 3] += v[3];
      }
    u32x4 o0, o1;
#pragma unroll
    for (int e = 0; e < 4; ++e) {
      o0[e] = pk2(bflo(r0[e]) + bfr(s[2 * e]), bfhi(r0[e]) + bfr(s[2 * e + 1]));
      o1[e] = pk2(bflo(r1[e]) + bfr(s[8 + 2 * e]), bfhi(r1[e]) + bfr(s[8 + 2 * e + 1]));
    }
    *(u32x4*)(OUT + go) = o0;
    *(u32x4*)(OUT + go + 8) = o1;
  }
  TS_MARK(1);
  TS_END(KT == 512 ? 2 : 4);
}

extern "C" void kernel_launch(void* const* d_in, const int* in_sizes, int n_in, void* d_out, int out_size,
                              void* d_ws, size_t ws_size, hipStream_t stream) {
  const u16* x = (const u16*)d_in[0];
  const u16* wqkv = (const u16*)d_in[1];
  const u16* wo = (const u16*)d_in[2];
  const u16* wg = (const u16*)d_in[4];
  const u16* wu = (const u16*)d_in[5];
  const u16* wd = (const u16*)d_in[6];
  u16* out = (u16*)d_out;
  char* ws = (char*)d_ws;
  const size_t MB2 = 2u << 20;
  u16* Qh = (u16*)(ws);
  u16* Kh = (u16*)(ws + MB2);
  u16* Vh = (u16*)(ws + 2 * MB2);
  u16* AO = (u16*)(ws + 3 * MB2);
  u16* Hb = (u16*)(ws + 4 * MB2);
  u16* Act = (u16*)(ws + 5 * MB2);

  ln_gemm_kernel<0><<<192, 512, 0, stream>>>(x, wqkv, wqkv, Qh, Kh, Vh);
  attn_kernel<<<256, 1024, 0, stream>>>(Qh, Kh, Vh, AO, wo, wg, wu, wd);
  gemm_res_kernel<512><<<256, 512, 0, stream>>>(AO, wo, x, Hb);
  ln_gemm_kernel<1><<<224, 512, 0, stream>>>(Hb, wg, wu, Act, Act, Act);
  gemm_res_kernel<1792><<<256, 512, 0, stream>>>(Act, wd, Hb, out);
}
```
